```python
import torch

def _softmax(x, axis=-1):
    return torch.softmax(x, dim=axis)
def _gelu(x):
    return torch.nn.functional.gelu(x, approximate="tanh")
def _silu(x):
    return torch.nn.functional.silu(x)
def _sigmoid(x):
    return torch.sigmoid(x)
def _layernorm(x, axis=-1, eps=1e-5):
    m = x.mean(dim=axis, keepdim=True)
    v = x.var(dim=axis, keepdim=True, unbiased=False)
    return (x - m) / torch.sqrt(v + eps)
def _scatter_add(tgt, idx, src, axis=0):
    return tgt.clone().index_add_(axis, idx, src)
def _segment_sum(src, seg, n):
    out = torch.zeros((n,) + src.shape[1:], dtype=src.dtype)
    return out.index_add_(0, seg, src)
def _pad(x, pw):
    flat = tuple(v for pair in reversed(pw) for v in pair)
    return torch.nn.functional.pad(x, flat)

kernel_name = 'sequential_preLN__gqa_block_cached__swiglu_ffn__decode'

def setup_inputs():

    _w = (lambda a: (lambda t: t.to(torch.bfloat16) if t.is_floating_point() else t)(torch.as_tensor(a)))
    def _A_setup():
        B, Sq, D = 4, 1, 512
        Hq, Hkv, Sk, Dh = 32, 8, 16384, 128
        assert Hq % Hkv == 0, (Hq, Hkv)
        return {
            "x": _w((lambda *s: torch.randn(*s, dtype=torch.bfloat16) * 0.05)(B, Sq, D)),
            "k_cache": _w((lambda *s: torch.randn(*s, dtype=torch.bfloat16) * 3.2)(B, Hkv, Sk, Dh)),
            "v_cache": _w((lambda *s: torch.randn(*s, dtype=torch.bfloat16) * 0.4)(B, Hkv, Sk, Dh)),
            "wq": _w((lambda *s: torch.randn(*s, dtype=torch.bfloat16) * 3.2)(D, Hq * Dh) * (D ** -0.5)),
            "wo": _w((lambda *s: torch.randn(*s, dtype=torch.bfloat16) * 0.4)(Hq * Dh, D) * ((Hq * Dh) ** -0.5)),
        }
    _A_in = _A_setup()

    def _B_setup():
        B, S, D, F = 4, 1, 512, 1792
        return {
            "x": _w((lambda *s: torch.randn(*s, dtype=torch.bfloat16) * 0.05)(B, S, D)),
            "w_gate": _w((lambda *s: torch.randn(*s, dtype=torch.bfloat16) * 0.05)(D, F)),
            "w_up": _w((lambda *s: torch.randn(*s, dtype=torch.bfloat16) * 0.05)(D, F)),
            "w_down": _w((lambda *s: torch.randn(*s, dtype=torch.bfloat16) * 0.05)(F, D)),
        }
    _B_in = _B_setup()

    return {
        "A_x": _A_in["x"],
        "A_k_cache": _A_in["k_cache"],
        "A_v_cache": _A_in["v_cache"],
        "A_wq": _A_in["wq"],
        "A_wo": _A_in["wo"],
        "B_x": _B_in["x"],
        "B_w_gate": _B_in["w_gate"],
        "B_w_up": _B_in["w_up"],
        "B_w_down": _B_in["w_down"],
    }

def reference(A_x, A_k_cache, A_v_cache, A_wq, A_wo, B_x, B_w_gate, B_w_up, B_w_down):
    def _A_ref(x, k_cache, v_cache, wq, wo):
        B, Sq, D = x.shape
        Hq, Dh = 32, 128
        Hkv = k_cache.shape[1]
        rep = Hq // Hkv
        q = (lambda x, *d: x.permute(*d))((x @ wq).reshape(B, Sq, Hq, Dh), 0, 2, 1, 3)
        k = (lambda x, n, axis: torch.repeat_interleave(x, n, dim=axis))(k_cache, rep, axis=1)
        v = (lambda x, n, axis: torch.repeat_interleave(x, n, dim=axis))(v_cache, rep, axis=1)
        scores = torch.einsum("bhqd,bhkd->bhqk", q, k) * (Dh ** -0.5)
        p = _softmax(scores, axis=-1)
        o = torch.einsum("bhqk,bhkd->bhqd", p, v)
        return (lambda x, *d: x.permute(*d))(o, 0, 2, 1, 3).reshape(B, Sq, Hq * Dh) @ wo

    def _B_ref(x, w_gate, w_up, w_down):
        g = _silu(x @ w_gate)
        u = x @ w_up
        return (g * u) @ w_down

    x = A_x
    h = x + _A_ref(_layernorm(x, axis=-1), A_k_cache, A_v_cache, A_wq, A_wo)
    h2 = h + _B_ref(_layernorm(h, axis=-1), B_w_gate, B_w_up, B_w_down)
    return h2
```

```cpp
#include <hip/hip_runtime.h>
#include <hip/hip_bf16.h>
#include <stdint.h>

#define MIMIC 1

typedef unsigned u32;
typedef unsigned u32x4 __attribute__((ext_vector_type(4)));
typedef float f32x4 __attribute__((ext_vector_type(4)));
typedef short bf16x8 __attribute__((ext_vector_type(8)));
typedef __bf16 bf16x2_t __attribute__((ext_vector_type(2)));
typedef float f32x2 __attribute__((ext_vector_type(2)));

constexpr int B = 4, D = 512, HQ = 32, HKV = 8, SK = 16384, DH = 128, F = 1792;
constexpr int NSPLIT = 8, KEYS_PER_WG = SK / NSPLIT;
constexpr int NW = 8;
constexpr int TILE = 32;
constexpr int TILES_PER_WAVE = KEYS_PER_WG / TILE / NW;

__device__ __forceinline__ float bf2f(unsigned short h) { return __builtin_bit_cast(float, (u32)h << 16); }
__device__ __forceinline__ float bflo(u32 w) { return __builtin_bit_cast(float, w << 16); }
__device__ __forceinline__ float bfhi(u32 w) { return __builtin_bit_cast(float, w & 0xffff0000u); }
__device__ __forceinline__ u32 cvtpk(float lo, float hi) {
  f32x2 v = {lo, hi};
  bf16x2_t r = __builtin_convertvector(v, bf16x2_t);
  return __builtin_bit_cast(u32, r);
}
__device__ __forceinline__ float rbf(float x) { return bflo(cvtpk(x, x)); }
__device__ __forceinline__ unsigned short f2bf(float x) { return (unsigned short)(cvtpk(x, x) & 0xffffu); }
__device__ __forceinline__ f32x4 mfma16(u32x4 a, u32x4 b, f32x4 c) {
  return __builtin_amdgcn_mfma_f32_16x16x32_bf16(__builtin_bit_cast(bf16x8, a), __builtin_bit_cast(bf16x8, b), c, 0, 0, 0);
}
__device__ __forceinline__ float wave_sum(float x) {
#pragma unroll
  for (int o = 32; o >= 1; o >>= 1) x += __shfl_xor(x, o);
  return x;
}
__device__ __forceinline__ float xg_max(float x) {
  auto s = __builtin_amdgcn_permlane16_swap(__float_as_uint(x), __float_as_uint(x), false, false);
  x = fmaxf(__uint_as_float(s[0]), __uint_as_float(s[1]));
  auto t = __builtin_amdgcn_permlane32_swap(__float_as_uint(x), __float_as_uint(x), false, false);
  return fmaxf(__uint_as_float(t[0]), __uint_as_float(t[1]));
}
__device__ __forceinline__ float xg_sum(float x) {
  auto s = __builtin_amdgcn_permlane16_swap(__float_as_uint(x), __float_as_uint(x), false, false);
  x = __uint_as_float(s[0]) + __uint_as_float(s[1]);
  auto t = __builtin_amdgcn_permlane32_swap(__float_as_uint(x), __float_as_uint(x), false, false);
  return __uint_as_float(t[0]) + __uint_as_float(t[1]);
}

__device__ __forceinline__ void ln8(float (&x)[8]) {
  float s = 0.f;
#pragma unroll
  for (int i = 0; i < 8; ++i) s += x[i];
  s = wave_sum(s);
  const float mean = s * (1.f / D);
  float v = 0.f;
#pragma unroll
  for (int i = 0; i < 8; ++i) { const float d = x[i] - mean; v += d * d; }
  v = wave_sum(v) * (1.f / D);
  const float mb = rbf(mean), vb = rbf(v);
  const float den = rbf(sqrtf(rbf(vb + 1e-5f)));
#pragma unroll
  for (int i = 0; i < 8; ++i) x[i] = rbf(rbf(x[i] - mb) / den);
}

__global__ void __launch_bounds__(256) k_qproj(const unsigned short* __restrict__ x, const unsigned short* __restrict__ wq, unsigned short* __restrict__ q) {
  __shared__ float xn[B][D];
  __shared__ float red[32][B][64];
  const int tid = threadIdx.x, lane = tid & 63, w = tid >> 6;
  {
    float v[8];
    const u32x4 raw = *(const u32x4*)(x + w * D + lane * 8);
#pragma unroll
    for (int i = 0; i < 4; ++i) { v[2 * i] = bflo(raw[i]); v[2 * i + 1] = bfhi(raw[i]); }
    ln8(v);
#pragma unroll
    for (int i = 0; i < 8; ++i) xn[w][lane * 8 + i] = v[i];
  }
  __syncthreads();
  const int rg = tid >> 3, cc = tid & 7, col0 = blockIdx.x * 64 + cc * 8;
  float acc[B][8] = {};
#pragma unroll
  for (int r = 0; r < 16; ++r) {
    const int row = r * 32 + rg;
    const u32x4 wv = *(const u32x4*)(wq + (size_t)row * (HQ * DH) + col0);
#pragma unroll
    for (int b = 0; b < B; ++b) {
      const float xv = xn[b][row];
#pragma unroll
      for (int i = 0; i < 4; ++i) { acc[b][2 * i] += xv * bflo(wv[i]); acc[b][2 * i + 1] += xv * bfhi(wv[i]); }
    }
  }
#pragma unroll
  for (int b = 0; b < B; ++b)
#pragma unroll
    for (int i = 0; i < 8; ++i) red[rg][b][cc * 8 + i] = acc[b][i];
  __syncthreads();
  {
    const int b = tid >> 6, c = tid & 63;
    float s = 0.f;
#pragma unroll
    for (int r = 0; r < 32; ++r) s += red[r][b][c];
    q[b * (HQ * DH) + blockIdx.x * 64 + c] = f2bf(s);
  }
}

constexpr int PREC = 132;
__global__ void __launch_bounds__(NW * 64) k_attn(const unsigned short* __restrict__ q, const unsigned short* __restrict__ kc,
                                                  const unsigned short* __restrict__ vc, float* __restrict__ part) {
  __shared__ float mrg[NW][4][PREC];
  const int tid = threadIdx.x, lane = tid & 63, w = __builtin_amdgcn_readfirstlane(tid >> 6);
  const int split = blockIdx.x & 7, bk = blockIdx.x >> 3;
  const int b = bk >> 3, kvh = bk & 7;
  const int g = lane >> 4, n = lane & 15;
  u32x4 qf[4];
#pragma unroll
  for (int s = 0; s < 4; ++s) qf[s] = *(const u32x4*)(q + b * (HQ * DH) + (kvh * 4 + (n & 3)) * DH + 32 * s + 8 * g);
  const size_t base = ((size_t)bk * SK + (size_t)split * KEYS_PER_WG) * DH;
  const char* kb = (const char*)(kc + base);
  const char* vb = (const char*)(vc + base);
  const int koff = (4 * (n & 3) + (n >> 2)) * 256 + g * 16;
  const int voff = lane * 16;
  float m = -INFINITY, l = 0.f;
  f32x4 acc[8];
#pragma unroll
  for (int e = 0; e < 8; ++e) acc[e] = f32x4{0.f, 0.f, 0.f, 0.f};
  u32x4 kn[8], vn[8];
  auto issue = [&](int i) {
    const int tile = w + NW * i;
    const char* kp = kb + (size_t)tile * 8192 + koff;
    const char* vp = vb + (size_t)tile * 8192 + voff;
#pragma unroll
    for (int t = 0; t < 2; ++t)
#pragma unroll
      for (int s = 0; s < 4; ++s) kn[t * 4 + s] = __builtin_nontemporal_load((const u32x4*)(kp + t * 4096 + s * 64));
#pragma unroll
    for (int j = 0; j < 8; ++j) vn[j] = __builtin_nontemporal_load((const u32x4*)(vp + j * 1024));
  };
  issue(0);
  constexpr float SCALE = 0.08838834764831845f, LOG2E = 1.4426950408889634f;
  for (int i = 0; i < TILES_PER_WAVE; ++i) {
    u32x4 kcur[8], vcur[8];
#pragma unroll
    for (int j = 0; j < 8; ++j) { kcur[j] = kn[j]; vcur[j] = vn[j]; }
    if (i + 1 < TILES_PER_WAVE) issue(i + 1);
    f32x4 s0 = {0.f, 0.f, 0.f, 0.f}, s1 = {0.f, 0.f, 0.f, 0.f};
#pragma unroll
    for (int s = 0; s < 4; ++s) { s0 = mfma16(kcur[s], qf[s], s0); s1 = mfma16(kcur[4 + s], qf[s], s1); }
    float sc[8];
#pragma unroll
    for (int r = 0; r < 4; ++r) { sc[r] = s0[r]; sc[4 + r] = s1[r]; }
#pragma unroll
    for (int r = 0; r < 8; r += 2) {
      const u32 p0 = cvtpk(sc[r], sc[r + 1]);
      const u32 p1 = cvtpk(bflo(p0) * SCALE, bfhi(p0) * SCALE);
      sc[r] = bflo(p1) * LOG2E; sc[r + 1] = bfhi(p1) * LOG2E;
    }
    float tm = fmaxf(fmaxf(fmaxf(sc[0], sc[1]), fmaxf(sc[2], sc[3])), fmaxf(fmaxf(sc[4], sc[5]), fmaxf(sc[6], sc[7])));
    tm = xg_max(tm);
    if (__any(tm > m)) {
      const float mn = fmaxf(m, tm);
      const float alpha = __builtin_amdgcn_exp2f(m - mn);
      l *= alpha;
#pragma unroll
      for (int e = 0; e < 8; ++e) acc[e] *= alpha;
      m = mn;
    }
    float p[8];
#pragma unroll
    for (int r = 0; r < 8; ++r) { p[r] = __builtin_amdgcn_exp2f(sc[r] - m); l += p[r]; }
    u32x4 pf = {cvtpk(p[0], p[1]), cvtpk(p[2], p[3]), cvtpk(p[4], p[5]), cvtpk(p[6], p[7])};
#pragma unroll
    for (int i2 = 0; i2 < 4; ++i2) {
      u32x4 flo, fhi;
#pragma unroll
      for (int mm = 0; mm < 4; ++mm) {
        flo[mm] = __builtin_amdgcn_perm(vcur[2 * mm + 1][i2], vcur[2 * mm][i2], 0x05040100u);
        fhi[mm] = __builtin_amdgcn_perm(vcur[2 * mm + 1][i2], vcur[2 * mm][i2], 0x07060302u);
      }
      acc[2 * i2] = mfma16(flo, pf, acc[2 * i2]);
      acc[2 * i2 + 1] = mfma16(fhi, pf, acc[2 * i2 + 1]);
    }
  }
  l = xg_sum(l);
  if (n < 4) {
#pragma unroll
    for (int r = 0; r < 4; ++r) {
      float* dst = &mrg[w][n][8 * (4 * g + r)];
      *(f32x4*)dst = f32x4{acc[0][r], acc[1][r], acc[2][r], acc[3][r]};
      *(f32x4*)(dst + 4) = f32x4{acc[4][r], acc[5][r], acc[6][r], acc[7][r]};
    }
    if (g == 0) { mrg[w][n][128] = m; mrg[w][n][129] = l; }
  }
  __syncthreads();
  {
    const int h = tid >> 7, d = tid & 127;
    float M = -INFINITY;
#pragma unroll
    for (int ww = 0; ww < NW; ++ww) M = fmaxf(M, mrg[ww][h][128]);
    float L = 0.f, o = 0.f;
#pragma unroll
    for (int ww = 0; ww < NW; ++ww) {
      const float wt = __builtin_amdgcn_exp2f(mrg[ww][h][128] - M);
      L += wt * mrg[ww][h][129]; o += wt * mrg[ww][h][d];
    }
    float* dst = part + ((size_t)(b * HQ + kvh * 4 + h) * NSPLIT + split) * PREC;
    dst[d] = o;
    if (d == 0) { dst[128] = M; dst[129] = L; }
  }
}

__global__ void __launch_bounds__(256) k_wo(const float* __restrict__ part, const unsigned short* __restrict__ wo, float* __restrict__ apart) {
  __shared__ float o[B][DH];
  __shared__ float red[32][B][64];
  const int tid = threadIdx.x, head = blockIdx.x >> 3, cs = blockIdx.x & 7;
  for (int idx = tid; idx < B * DH; idx += 256) {
    const int b = idx >> 7, d = idx & 127;
    const float* pp = part + (size_t)(b * HQ + head) * NSPLIT * PREC;
    float M = -INFINITY;
#pragma unroll
    for (int s = 0; s < NSPLIT; ++s) M = fmaxf(M, pp[s * PREC + 128]);
    float L = 0.f, acc = 0.f;
#pragma unroll
    for (int s = 0; s < NSPLIT; ++s) { const float wt = __builtin_amdgcn_exp2f(pp[s * PREC + 128] - M); L += wt * pp[s * PREC + 129]; acc += wt * pp[s * PREC + d]; }
    const float ov = acc / L;
    o[b][d] = MIMIC ? rbf(ov) : ov;
  }
  __syncthreads();
  const int rg = tid >> 3, cc = tid & 7, col0 = cs * 64 + cc * 8;
  float acc[B][8] = {};
#pragma unroll
  for (int r = 0; r < 4; ++r) {
    const int row = r * 32 + rg;
    const u32x4 wv = *(const u32x4*)(wo + (size_t)(head * DH + row) * D + col0);
#pragma unroll
    for (int b = 0; b < B; ++b) {
      const float xv = o[b][row];
#pragma unroll
      for (int i = 0; i < 4; ++i) { acc[b][2 * i] += xv * bflo(wv[i]); acc[b][2 * i + 1] += xv * bfhi(wv[i]); }
    }
  }
#pragma unroll
  for (int b = 0; b < B; ++b)
#pragma unroll
    for (int i = 0; i < 8; ++i) red[rg][b][cc * 8 + i] = acc[b][i];
  __syncthreads();
  {
    const int b = tid >> 6, c = tid & 63;
    float s = 0.f;
#pragma unroll
    for (int r = 0; r < 32; ++r) s += red[r][b][c];
    apart[(size_t)(head * B + b) * D + cs * 64 + c] = s;
  }
}

__global__ void __launch_bounds__(256) k_ffn(const unsigned short* __restrict__ x, const float* __restrict__ apart,
                                             const unsigned short* __restrict__ wg, const unsigned short* __restrict__ wu,
                                             const unsigned short* __restrict__ wd, float* __restrict__ hout, float* __restrict__ ypart) {
  __shared__ float hn[B][D];
  __shared__ float red[64][2][B][32];
  __shared__ float act[B][32];
  const int tid = threadIdx.x, lane = tid & 63, w = tid >> 6;
  {
    float v[8];
    const u32x4 raw = *(const u32x4*)(x + w * D + lane * 8);
    float a[8] = {};
    for (int hd = 0; hd < HQ; ++hd) {
      const f32x4 a0 = *(const f32x4*)(apart + (size_t)(hd * B + w) * D + lane * 8);
      const f32x4 a1 = *(const f32x4*)(apart + (size_t)(hd * B + w) * D + lane * 8 + 4);
#pragma unroll
      for (int i = 0; i < 4; ++i) { a[i] += a0[i]; a[4 + i] += a1[i]; }
    }
#pragma unroll
    for (int i = 0; i < 4; ++i) { v[2 * i] = bflo(raw[i]); v[2 * i + 1] = bfhi(raw[i]); }
#pragma unroll
    for (int i = 0; i < 8; ++i) v[i] = MIMIC ? rbf(v[i] + rbf(a[i])) : v[i] + a[i];
    if (blockIdx.x == 0) {
#pragma unroll
      for (int i = 0; i < 8; ++i) hout[w * D + lane * 8 + i] = v[i];
    }
    ln8(v);
#pragma unroll
    for (int i = 0; i < 8; ++i) hn[w][lane * 8 + i] = v[i];
  }
  __syncthreads();
  const int rg = tid >> 2, cc = tid & 3, col0 = blockIdx.x * 32 + cc * 8;
  float ag[B][8] = {}, au[B][8] = {};
#pragma unroll
  for (int r = 0; r < 8; ++r) {
    const int row = r * 64 + rg;
    const u32x4 gv = *(const u32x4*)(wg + (size_t)row * F + col0);
    const u32x4 uv = *(const u32x4*)(wu + (size_t)row * F + col0);
#pragma unroll
    for (int b = 0; b < B; ++b) {
      const float xv = hn[b][row];
#pragma unroll
      for (int i = 0; i < 4; ++i) {
        ag[b][2 * i] += xv * bflo(gv[i]); ag[b][2 * i + 1] += xv * bfhi(gv[i]);
        au[b][2 * i] += xv * bflo(uv[i]); au[b][2 * i + 1] += xv * bfhi(uv[i]);
      }
    }
  }
#pragma unroll
  for (int b = 0; b < B; ++b)
#pragma unroll
    for (int i = 0; i < 8; ++i) { red[rg][0][b][cc * 8 + i] = ag[b][i]; red[rg][1][b][cc * 8 + i] = au[b][i]; }
  __syncthreads();
  if (tid < 128) {
    const int b = tid >> 5, c = tid & 31;
    float gs = 0.f, us = 0.f;
    for (int r = 0; r < 64; ++r) { gs += red[r][0][b][c]; us += red[r][1][b][c]; }
    gs = rbf(gs); us = rbf(us);
    const float sg = rbf(gs / (1.f + __expf(-gs)));
    act[b][c] = rbf(sg * us);
  }
  __syncthreads();
  {
    float y[B][2] = {};
#pragma unroll 8
    for (int r = 0; r < 32; ++r) {
      const u32 wv = *(const u32*)(wd + (size_t)(blockIdx.x * 32 + r) * D + tid * 2);
#pragma unroll
      for (int b = 0; b < B; ++b) { y[b][0] += act[b][r] * bflo(wv); y[b][1] += act[b][r] * bfhi(wv); }
    }
#pragma unroll
    for (int b = 0; b < B; ++b) *(f32x2*)(ypart + (size_t)(blockIdx.x * B + b) * D + tid * 2) = f32x2{y[b][0], y[b][1]};
  }
}

__global__ void __launch_bounds__(256) k_out(const float* __restrict__ hin, const float* __restrict__ ypart, unsigned short* __restrict__ out) {
  const int idx = blockIdx.x * 256 + threadIdx.x;
  float s = 0.f;
#pragma unroll 8
  for (int p = 0; p < F / 32; ++p) s += ypart[(size_t)p * B * D + idx];
  out[idx] = f2bf(hin[idx] + (MIMIC ? rbf(s) : s));
}

extern "C" void kernel_launch(void* const* d_in, const int* in_sizes, int n_in, void* d_out, int out_size, void* d_ws, size_t ws_size,
                              hipStream_t stream) {
  const unsigned short* x = (const unsigned short*)d_in[0];
  const unsigned short* kc = (const unsigned short*)d_in[1];
  const unsigned short* vc = (const unsigned short*)d_in[2];
  const unsigned short* wq = (const unsigned short*)d_in[3];
  const unsigned short* wo = (const unsigned short*)d_in[4];
  const unsigned short* wg = (const unsigned short*)d_in[6];
  const unsigned short* wu = (const unsigned short*)d_in[7];
  const unsigned short* wd = (const unsigned short*)d_in[8];
  char* ws = (char*)d_ws;
  unsigned short* q = (unsigned short*)ws;
  float* part = (float*)(ws + 65536);
  float* apart = (float*)(ws + 65536 + 1048576);
  float* h = (float*)(ws + 65536 + 2 * 1048576);
  float* ypart = (float*)(ws + 65536 + 3 * 1048576);
  k_qproj<<<64, 256, 0, stream>>>(x, wq, q);
  k_attn<<<B * HKV * NSPLIT, NW * 64, 0, stream>>>(q, kc, vc, part);
  k_wo<<<256, 256, 0, stream>>>(part, wo, apart);
  k_ffn<<<F / 32, 256, 0, stream>>>(x, apart, wg, wu, wd, h, ypart);
  k_out<<<8, 256, 0, stream>>>(h, ypart, (unsigned short*)d_out);
}
```

```cpp
#include <hip/hip_runtime.h>
#include <stdint.h>

typedef unsigned u32;
typedef unsigned long long u64;
typedef unsigned u32x2 __attribute__((ext_vector_type(2)));
typedef unsigned u32x4 __attribute__((ext_vector_type(4)));
typedef float f32x2 __attribute__((ext_vector_type(2)));
typedef float f32x4 __attribute__((ext_vector_type(4)));
typedef short bf16x8 __attribute__((ext_vector_type(8)));
typedef short s16x4 __attribute__((ext_vector_type(4)));
typedef __bf16 bf16x2_t __attribute__((ext_vector_type(2)));
typedef __amdgpu_buffer_rsrc_t rsrc_t;
#define LAS __attribute__((address_space(3)))

constexpr int NWG = 256, NTHR = 512;
constexpr int AUX_NT = 2, AUX_SC1 = 16;
#define WQ_AUX 2
#define WT_AUX 2

constexpr int WS_E = 0;
constexpr int WS_Q = 4096;
constexpr int PREC = 1152;
constexpr int WS_P = WS_Q + 4 * 4096 * 8;
constexpr int WS_A = WS_P + 128 * 8 * PREC;
constexpr int WS_H = WS_A + 128 * 32 * 128;
constexpr int WS_Y = WS_H + 2048 * 8;
constexpr int WS_END = WS_Y + 128 * 56 * 128;
#define STAMP(i) do { } while (0)

constexpr int WOSTR = 288, WDSTR = 1056;
constexpr int L_W = 0;
constexpr int L_WG = 0, L_WU = 32768, L_WD = 65536;
constexpr int L_MRG = L_WD + 32 * WDSTR;
constexpr int L_QRED = L_MRG + 8 * 4 * 132 * 4;
constexpr int L_QS = L_QRED + 2048;
constexpr int L_OB = L_QS + 1280;
constexpr int L_HN = L_OB + 1280;
constexpr int L_RED = L_HN + 4 * 1088;
constexpr int L_ACT = L_RED + 2048;
constexpr int LDS_BYTES = L_ACT + 256;

constexpr u32 SPIN_CAP = 1u << 16;

struct Params {
  const void* x; const void* k; const void* v; const void* wq; const void* wo; const void* wg; const void* wu; const void* wd;
  void* out; void* ws;
};

__device__ __forceinline__ float bflo(u32 w) { return __builtin_bit_cast(float, w << 16); }
__device__ __forceinline__ float bfhi(u32 w) { return __builtin_bit_cast(float, w & 0xffff0000u); }
__device__ __forceinline__ u32 cvtpk(float lo, float hi) {
  f32x2 v = {lo, hi};
  return __builtin_bit_cast(u32, __builtin_convertvector(v, bf16x2_t));
}
__device__ __forceinline__ float rbf(float x) { return bflo(cvtpk(x, x)); }
__device__ __forceinline__ f32x4 mfma16(u32x4 a, u32x4 b, f32x4 c) {
  return __builtin_amdgcn_mfma_f32_16x16x32_bf16(__builtin_bit_cast(bf16x8, a), __builtin_bit_cast(bf16x8, b), c, 0, 0, 0);
}
template <int CTRL> __device__ __forceinline__ float dpp(float x) {
  return __builtin_bit_cast(float, __builtin_amdgcn_mov_dpp(__builtin_bit_cast(int, x), CTRL, 0xf, 0xf, true));
}
constexpr int XOR1 = 0xB1, XOR2 = 0x4E, ROR4 = 0x124, ROR8 = 0x128;
__device__ __forceinline__ float x16_32_max(float x) {
  auto s = __builtin_amdgcn_permlane16_swap(__float_as_uint(x), __float_as_uint(x), false, false);
  x = fmaxf(__uint_as_float(s[0]), __uint_as_float(s[1]));
  auto t = __builtin_amdgcn_permlane32_swap(__float_as_uint(x), __float_as_uint(x), false, false);
  return fmaxf(__uint_as_float(t[0]), __uint_as_float(t[1]));
}
__device__ __forceinline__ float x16_32_sum(float x) {
  auto s = __builtin_amdgcn_permlane16_swap(__float_as_uint(x), __float_as_uint(x), false, false);
  x = __uint_as_float(s[0]) + __uint_as_float(s[1]);
  auto t = __builtin_amdgcn_permlane32_swap(__float_as_uint(x), __float_as_uint(x), false, false);
  return __uint_as_float(t[0]) + __uint_as_float(t[1]);
}
__device__ __forceinline__ float sum_b345(float x) { x += dpp<ROR8>(x); return x16_32_sum(x); }
__device__ __forceinline__ float max_b345(float x) { x = fmaxf(x, dpp<ROR8>(x)); return x16_32_max(x); }
__device__ __forceinline__ float sum_b2345(float x) { x += dpp<ROR4>(x); return sum_b345(x); }
__device__ __forceinline__ float sum_all(float x) { x += dpp<XOR1>(x); x += dpp<XOR2>(x); return sum_b2345(x); }

__device__ __forceinline__ rsrc_t mk_rsrc(const void* p, u32 bytes) { return __builtin_amdgcn_make_buffer_rsrc((void*)p, 0, bytes, 0x00020000); }
template <int AUX> __device__ __forceinline__ u32x4 ld128(rsrc_t r, int vo, int so) { return __builtin_amdgcn_raw_buffer_load_b128(r, vo, so, AUX); }
template <int AUX> __device__ __forceinline__ u32x2 ld64(rsrc_t r, int vo, int so) { return __builtin_amdgcn_raw_buffer_load_b64(r, vo, so, AUX); }
template <int AUX> __device__ __forceinline__ u32 ld32(rsrc_t r, int vo, int so) { return __builtin_amdgcn_raw_buffer_load_b32(r, vo, so, AUX); }
__device__ __forceinline__ void st128_sc1(u32x4 v, rsrc_t r, int vo, int so) { __builtin_amdgcn_raw_buffer_store_b128(v, r, vo, so, AUX_SC1); }
__device__ __forceinline__ void st64_sc1(u32x2 v, rsrc_t r, int vo, int so) { __builtin_amdgcn_raw_buffer_store_b64(v, r, vo, so, AUX_SC1); }

#define BAR() asm volatile("s_waitcnt lgkmcnt(0)\n\ts_barrier" ::: "memory")
#define FENCE() asm volatile("" ::: "memory")
#define SCHED() __builtin_amdgcn_sched_barrier(0)

struct Tag { u32 lo, hi; };
__device__ __forceinline__ u32 gpay(float v, Tag t) { return (__float_as_uint(v) & 0xffffff00u) | t.hi; }
__device__ __forceinline__ u32x4 gpair(float a, float b, Tag t) { return u32x4{gpay(a, t), t.lo, gpay(b, t), t.lo}; }
__device__ __forceinline__ bool gok(u32 pay, u32 tl, Tag t) { return tl == t.lo && (pay & 0xffu) == t.hi; }
__device__ __forceinline__ bool gok2(u32x4 g, Tag t) { return gok(g[0], g[1], t) && gok(g[2], g[3], t); }
__device__ __forceinline__ float gval(u32 pay) { return __uint_as_float(pay & 0xffffff00u); }

__device__ __forceinline__ u32x2 tr_read(char* smem, int off) {
  return __builtin_bit_cast(u32x2, __builtin_amdgcn_ds_read_tr16_b64_v4i16((LAS s16x4*)(smem + off)));
}
__device__ __forceinline__ u32x4 tr_frag(char* smem, int off, int str, int g, int n) {
  const int o = off + (8 * g + (n >> 2)) * str + (n & 3) * 8;
  const u32x2 a = tr_read(smem, o), b = tr_read(smem, o + 4 * str);
  return u32x4{a[0], a[1], b[0], b[1]};
}

__global__ void __launch_bounds__(NTHR, 2) fused_block(Params p) {
  __shared__ __attribute__((aligned(16))) char smem[LDS_BYTES];
  const int c = blockIdx.x, tid = threadIdx.x, lane = tid & 63, w = __builtin_amdgcn_readfirstlane(tid >> 6);
  const int g = lane >> 4, n = lane & 15;
  const int kvh = c & 7, rest = c >> 3, b = rest >> 3, split = rest & 7;
  const int ridx = (c & 7) + 8 * (c >> 4);
  const bool roleA = (rest & 1) == 0;
  const bool roleB = !roleA && (c >> 4) < 7;
  STAMP(0);

  const rsrc_t rws = mk_rsrc(p.ws, WS_END);
  const rsrc_t rx = mk_rsrc(p.x, 4096);
  const rsrc_t rk = mk_rsrc(p.k, 128u << 20);
  const rsrc_t rv = mk_rsrc(p.v, 128u << 20);

  const u32x2 eraw = ld64<AUX_SC1>(rws, 0, WS_E);
  u32x4 xs[4], xb[2], wqr[2];
  {
    const int bb = n & 3, i16 = (n >> 2) + 4 * g;
#pragma unroll
    for (int i = 0; i < 4; ++i) xs[i] = ld128<0>(rx, bb * 1024 + i16 * 64 + i * 16, 0);
#pragma unroll
    for (int s = 0; s < 2; ++s) xb[s] = ld128<0>(rx, bb * 1024 + g * 16 + s * 64, w * 128);
    const rsrc_t rwq = mk_rsrc(p.wq, 4u << 20);
#pragma unroll
    for (int i = 0; i < 2; ++i) wqr[i] = ld128<WQ_AUX>(rwq, (lane >> 1) * 8192 + (lane & 1) * 16, (64 * w + 32 * i) * 8192 + (kvh * 512 + rest * 16) * 2);
  }
  const int kvo = (4 * (n & 3) + (n >> 2)) * 256 + g * 16, vvo = lane * 16;
  const int sbase = ((b * 8 + kvh) * 16384 + split * 2048) * 256 + w * 8192;
  u32x4 K0[8], V0[8], K1[8], V1[8];
  auto ldK = [&](u32x4 (&kk)[8], int tile, int j0, int j1) {
#pragma unroll
    for (int j = 0; j < 8; ++j) if (j >= j0 && j < j1) kk[j] = ld128<AUX_NT>(rk, kvo + (j & 3) * 64, sbase + tile * 65536 + (j >> 2) * 4096);
  };
  auto ldV = [&](u32x4 (&vv)[8], int tile) {
#pragma unroll
    for (int j = 0; j < 8; ++j) vv[j] = ld128<AUX_NT>(rv, vvo + (j & 3) * 1024, sbase + tile * 65536 + (j >> 2) * 4096);
  };
  FENCE(); SCHED();
  ldK(K0, 0, 0, 4);
  FENCE(); SCHED();

  Tag tag;
  {
    float s = 0.f;
#pragma unroll
    for (int i = 0; i < 4; ++i)
#pragma unroll
      for (int e = 0; e < 4; ++e) s += bflo(xs[i][e]) + bfhi(xs[i][e]);
    const float mean = sum_b2345(s) * (1.f / 512);
    float v = 0.f;
#pragma unroll
    for (int i = 0; i < 4; ++i)
#pragma unroll
      for (int e = 0; e < 4; ++e) { const float d0 = bflo(xs[i][e]) - mean, d1 = bfhi(xs[i][e]) - mean; v += d0 * d0; v += d1 * d1; }
    v = sum_b2345(v) * (1.f / 512);
    const float mb = rbf(mean), den = rbf(sqrtf(rbf(rbf(v) + 1e-5f)));
    u32x4 xn[2];
#pragma unroll
    for (int s2 = 0; s2 < 2; ++s2)
#pragma unroll
      for (int e = 0; e < 4; ++e) {
        const u32 d = cvtpk(bflo(xb[s2][e]) - mb, bfhi(xb[s2][e]) - mb);
        xn[s2][e] = cvtpk(bflo(d) / den, bfhi(d) / den);
      }
    const u64 e1 = (((u64)(u32)__builtin_amdgcn_readfirstlane(eraw[1]) << 32) | (u32)__builtin_amdgcn_readfirstlane(eraw[0])) + 1ull;
    tag.lo = (u32)e1; tag.hi = (u32)(e1 >> 32) & 0xffu;
    char* wl = smem + L_W + w * 2048;
    *(u32x4*)(wl + lane * 16) = wqr[0];
    *(u32x4*)(wl + 1024 + lane * 16) = wqr[1];
    STAMP(1);
    f32x4 qa = {0.f, 0.f, 0.f, 0.f};
    qa = mfma16(tr_frag(wl, 0, 32, g, n), xn[0], qa);
    qa = mfma16(tr_frag(wl, 1024, 32, g, n), xn[1], qa);
    if (n < 4) *(f32x4*)(smem + L_QRED + (w * 64 + n * 16 + 4 * g) * 4) = qa;
    BAR();
    if (w == 0) {
      float q = 0.f;
#pragma unroll
      for (int ww = 0; ww < 8; ++ww) q += *(const float*)(smem + L_QRED + (ww * 64 + lane) * 4);
      st64_sc1(u32x2{gpay(rbf(q), tag), tag.lo}, rws, (g * 4096 + n) * 8, WS_Q + (kvh * 512 + rest * 16) * 8);
    }
    BAR();
    STAMP(2);
  }
  ldK(K0, 0, 4, 8);
  ldV(V0, 0);
  FENCE(); SCHED();
  asm volatile("s_waitcnt vmcnt(8)" ::: "memory");
  STAMP(3);
  const u32x2 gq = ld64<AUX_SC1>(rws, tid * 8, WS_Q + (b * 4096 + kvh * 512) * 8);
  FENCE(); SCHED();
  ldK(K1, 1, 0, 8);
  ldV(V1, 1);
  FENCE(); SCHED();
  u32 qv = gq[0];
  if (!__all(gok(gq[0], gq[1], tag))) {
    for (u32 spin = 0; spin < SPIN_CAP; ++spin) {
      FENCE();
      const u32x2 g2 = ld64<AUX_SC1>(rws, tid * 8, WS_Q + (b * 4096 + kvh * 512) * 8);
      qv = g2[0];
      if (__all(gok(g2[0], g2[1], tag))) break;
    }
  }
  STAMP(4);
  *(unsigned short*)(smem + L_QS + (tid >> 7) * 320 + (tid & 127) * 2) = (unsigned short)(qv >> 16);
  BAR();
  u32x4 qf[4];
#pragma unroll
  for (int s = 0; s < 4; ++s) qf[s] = *(const u32x4*)(smem + L_QS + (n & 3) * 320 + s * 64 + g * 16);
  STAMP(5);

  constexpr float SCALE = 0.08838834764831845f, LOG2E = 1.4426950408889634f;
  float m = -INFINITY, l = 0.f;
  f32x4 acc[8];
#pragma unroll
  for (int e = 0; e < 8; ++e) acc[e] = f32x4{0.f, 0.f, 0.f, 0.f};
  u32x4 wr[12];
  const int head = ridx >> 2, quarter = ridx & 3, pB = roleB ? 7 * (c & 7) + (c >> 4) : 0;
  auto step = [&](u32x4 (&kk)[8], u32x4 (&vv)[8], int next) {
    f32x4 s0 = {0.f, 0.f, 0.f, 0.f}, s1 = {0.f, 0.f, 0.f, 0.f};
#pragma unroll
    for (int s = 0; s < 4; ++s) { s0 = mfma16(kk[s], qf[s], s0); s1 = mfma16(kk[4 + s], qf[s], s1); }
    SCHED();
    if (next < 8) ldK(kk, next, 0, 8);
    SCHED();
    float t[8];
#pragma unroll
    for (int r = 0; r < 4; r += 2) {
      const u32 a0 = cvtpk(s0[r], s0[r + 1]), a1 = cvtpk(s1[r], s1[r + 1]);
      const u32 b0 = cvtpk(bflo(a0) * SCALE, bfhi(a0) * SCALE), b1 = cvtpk(bflo(a1) * SCALE, bfhi(a1) * SCALE);
      t[r] = bflo(b0); t[r + 1] = bfhi(b0); t[4 + r] = bflo(b1); t[5 + r] = bfhi(b1);
    }
    float tm = fmaxf(fmaxf(fmaxf(t[0], t[1]), fmaxf(t[2], t[3])), fmaxf(fmaxf(t[4], t[5]), fmaxf(t[6], t[7])));
    tm = x16_32_max(tm);
    if (__any(tm > m)) {
      const float mn = fmaxf(m, tm);
      const float alpha = __builtin_amdgcn_exp2f((m - mn) * LOG2E);
      l *= alpha;
#pragma unroll
      for (int e = 0; e < 8; ++e) acc[e] *= alpha;
      m = mn;
    }
    float pr[8];
#pragma unroll
    for (int r = 0; r < 8; ++r) { pr[r] = __builtin_amdgcn_exp2f((t[r] - m) * LOG2E); l += pr[r]; }
    const u32x4 pf = {cvtpk(pr[0], pr[1]), cvtpk(pr[2], pr[3]), cvtpk(pr[4], pr[5]), cvtpk(pr[6], pr[7])};
#pragma unroll
    for (int i = 0; i < 4; ++i) {
      u32x4 flo, fhi;
#pragma unroll
      for (int q = 0; q < 4; ++q) {
        flo[q] = __builtin_amdgcn_perm(vv[2 * q + 1][i], vv[2 * q][i], 0x05040100u);
        fhi[q] = __builtin_amdgcn_perm(vv[2 * q + 1][i], vv[2 * q][i], 0x07060302u);
      }
      acc[2 * i] = mfma16(flo, pf, acc[2 * i]);
      acc[2 * i + 1] = mfma16(fhi, pf, acc[2 * i + 1]);
    }
    SCHED();
    if (next < 8) ldV(vv, next);
    SCHED();
  };
#pragma unroll 1
  for (int i = 0; i < 6; i += 2) { step(K0, V0, i + 2); step(K1, V1, i + 3); }
  step(K0, V0, 8);
  {
    const rsrc_t rw = mk_rsrc(roleA ? p.wo : p.wd, roleA ? (4u << 20) : roleB ? 1792u * 512 * 2 : 0u);
    const int vo = roleA ? g * 1024 + n * 16 : lane * 16;
    const int so = roleA ? (head * 128 + 16 * w) * 1024 + quarter * 256 : (32 * pB + 4 * w) * 1024, st = roleA ? 4096 : 1024;
#pragma unroll
    for (int i = 0; i < 4; ++i) wr[8 + i] = ld128<WT_AUX>(rw, vo, so + i * st);
  }
  SCHED();
  step(K1, V1, 8);
  STAMP(6);

  l = x16_32_sum(l);
  {
    float* mrg = (float*)(smem + L_MRG);
    if (n < 4) {
      float* rec = mrg + (w * 4 + n) * 132;
#pragma unroll
      for (int r = 0; r < 4; ++r) {
        *(f32x4*)(rec + 8 * (4 * g + r)) = f32x4{acc[0][r], acc[1][r], acc[2][r], acc[3][r]};
        *(f32x4*)(rec + 8 * (4 * g + r) + 4) = f32x4{acc[4][r], acc[5][r], acc[6][r], acc[7][r]};
      }
      if (g == 0) *(f32x2*)(rec + 128) = f32x2{m, l};
    }
    BAR();
    if (w < 4) {
      f32x2 ml[8], ov[8];
#pragma unroll
      for (int ww = 0; ww < 8; ++ww) { ml[ww] = *(const f32x2*)(mrg + (ww * 4 + w) * 132 + 128); ov[ww] = *(const f32x2*)(mrg + (ww * 4 + w) * 132 + 2 * lane); }
      float M = ml[0][0];
#pragma unroll
      for (int ww = 1; ww < 8; ++ww) M = fmaxf(M, ml[ww][0]);
      float L = 0.f, o0 = 0.f, o1 = 0.f;
#pragma unroll
      for (int ww = 0; ww < 8; ++ww) {
        const float wt = __builtin_amdgcn_exp2f((ml[ww][0] - M) * LOG2E);
        L += wt * ml[ww][1]; o0 += wt * ov[ww][0]; o1 += wt * ov[ww][1];
      }
      const int so = WS_P + ((b * 32 + kvh * 4 + w) * 8 + split) * PREC;
      st128_sc1(gpair(o0, o1, tag), rws, lane * 16, so);
      if (lane == 0) st128_sc1(gpair(M, L, tag), rws, 1024, so);
    }
  }
  STAMP(7);

  if (roleA) {
    const rsrc_t rout = mk_rsrc(p.out, 4096);
    const int bR = ridx >> 5, tR = ridx & 31, hg = lane >> 3, j = lane & 7;
    const u32 xres = ld32<0>(rx, (bR * 512 + 16 * tR + 2 * j) * 2, 0);
#pragma unroll
    for (int i = 0; i < 4; ++i) *(u32x4*)(smem + L_W + (16 * w + 4 * i + g) * WOSTR + n * 16) = wr[8 + i];
    BAR();
    u32x4 af[4];
#pragma unroll
    for (int s = 0; s < 4; ++s) af[s] = tr_frag(smem, L_W + 32 * s * WOSTR + w * 32, WOSTR, g, n);
    {
      const int bA = w >> 1, dh = w & 1;
      const int so = WS_P + (bA * 32 + head) * 8 * PREC;
      const int vo = hg * PREC;
      u32x4 gml = {0, 0, 0, 0}, gv[4] = {{0, 0, 0, 0}, {0, 0, 0, 0}, {0, 0, 0, 0}, {0, 0, 0, 0}};
      bool done = false, seen = false;
      for (u32 spin = 0; spin < SPIN_CAP; ++spin) {
        FENCE();
        const int missing = __builtin_popcountll(__ballot(!done));
        if (missing == 0) break;
        if (!done) {
          const bool full = seen || missing <= 16;
          gml = ld128<AUX_SC1>(rws, vo + 1024, so);
          if (full) {
#pragma unroll
            for (int i = 0; i < 4; ++i) gv[i] = ld128<AUX_SC1>(rws, vo + dh * 512 + i * 128 + j * 16, so);
          }
          seen = gok2(gml, tag);
          done = full && seen && gok2(gv[0], tag) && gok2(gv[1], tag) && gok2(gv[2], tag) && gok2(gv[3], tag);
        }
      }
      STAMP(8);
      const float ms = gval(gml[0]), ls = gval(gml[2]);
      const float M = max_b345(ms);
      const float wt = __builtin_amdgcn_exp2f((ms - M) * LOG2E);
      const float inv = 1.f / sum_b345(wt * ls);
      u32 ob[4];
#pragma unroll
      for (int i = 0; i < 4; ++i) ob[i] = cvtpk(sum_b345(wt * gval(gv[i][0])) * inv, sum_b345(wt * gval(gv[i][2])) * inv);
      if (hg == 0) {
#pragma unroll
        for (int i = 0; i < 4; ++i) *(u32*)(smem + L_OB + bA * 320 + (dh * 64 + 16 * i + 2 * j) * 2) = ob[i];
      }
    }
    BAR();
    f32x4 aa = {0.f, 0.f, 0.f, 0.f};
#pragma unroll
    for (int s = 0; s < 4; ++s) aa = mfma16(af[s], *(const u32x4*)(smem + L_OB + (n & 3) * 320 + s * 64 + g * 16), aa);
    if (n < 4) {
      const int vo = n * 32 * 4096 + g * 32, so = WS_A + (quarter * 8 + w) * 4096 + head * 128;
      st128_sc1(gpair(aa[0], aa[1], tag), rws, vo, so);
      st128_sc1(gpair(aa[2], aa[3], tag), rws, vo + 16, so);
    }
    STAMP(9);
    if (w != 0) return;
    float h0, h1;
    {
      u32x4 ga[4];
      bool ok[4] = {false, false, false, false};
      for (u32 spin = 0; spin < SPIN_CAP; ++spin) {
        FENCE();
#pragma unroll
        for (int i = 0; i < 4; ++i) if (!ok[i]) ga[i] = ld128<AUX_SC1>(rws, hg * 128 + j * 16 + i * 1024, WS_A + ridx * 4096);
#pragma unroll
        for (int i = 0; i < 4; ++i) ok[i] = ok[i] || gok2(ga[i], tag);
        if (__all(ok[0] && ok[1] && ok[2] && ok[3])) break;
      }
      STAMP(10);
      const float a0 = sum_b345((gval(ga[0][0]) + gval(ga[1][0])) + (gval(ga[2][0]) + gval(ga[3][0])));
      const float a1 = sum_b345((gval(ga[0][2]) + gval(ga[1][2])) + (gval(ga[2][2]) + gval(ga[3][2])));
      h0 = rbf(bflo(xres) + rbf(a0)); h1 = rbf(bfhi(xres) + rbf(a1));
      if (hg == 0) st128_sc1(gpair(h0, h1, tag), rws, j * 16, WS_H + (bR * 512 + 16 * tR) * 8);
    }
    {
      u32x4 gy[7];
      bool ok[7] = {false, false, false, false, false, false, false};
      for (u32 spin = 0; spin < SPIN_CAP; ++spin) {
        FENCE();
#pragma unroll
        for (int i = 0; i < 7; ++i) if (!ok[i]) gy[i] = ld128<AUX_SC1>(rws, hg * 128 + j * 16 + (i & 3) * 1024, WS_Y + ridx * 7168 + (i >> 2) * 4096);
        bool all = true;
#pragma unroll
        for (int i = 0; i < 7; ++i) { ok[i] = ok[i] || gok2(gy[i], tag); all = all && ok[i]; }
        if (__all(all)) break;
      }
      STAMP(14);
      float y0 = 0.f, y1 = 0.f;
#pragma unroll
      for (int i = 0; i < 7; ++i) { y0 += gval(gy[i][0]); y1 += gval(gy[i][2]); }
      y0 = sum_b345(y0); y1 = sum_b345(y1);
      if (hg == 0) __builtin_amdgcn_raw_buffer_store_b32(cvtpk(h0 + rbf(y0), h1 + rbf(y1)), rout, (bR * 512 + 16 * tR + 2 * j) * 2, 0, 0);
      if (ridx == 0 && lane == 0) {
        const u64 e1 = (((u64)eraw[1] << 32) | eraw[0]) + 1ull;
        st64_sc1(u32x2{(u32)e1, (u32)(e1 >> 32)}, rws, 0, WS_E);
      }
    }
    STAMP(15);
  } else if (roleB) {
    {
      const rsrc_t rwg = mk_rsrc(p.wg, 512 * 1792 * 2), rwu = mk_rsrc(p.wu, 512 * 1792 * 2);
#pragma unroll
      for (int i = 0; i < 4; ++i) wr[i] = ld128<WT_AUX>(rwg, (lane >> 2) * 3584 + (lane & 3) * 16, (64 * w + 16 * i) * 3584 + pB * 64);
#pragma unroll
      for (int i = 0; i < 4; ++i) wr[4 + i] = ld128<WT_AUX>(rwu, (lane >> 2) * 3584 + (lane & 3) * 16, (64 * w + 16 * i) * 3584 + pB * 64);
#pragma unroll
      for (int i = 0; i < 4; ++i) {
        *(u32x4*)(smem + L_WG + (4 * w + i) * 1024 + lane * 16) = wr[i];
        *(u32x4*)(smem + L_WU + (4 * w + i) * 1024 + lane * 16) = wr[4 + i];
        *(u32x4*)(smem + L_WD + (4 * w + i) * WDSTR + lane * 16) = wr[8 + i];
      }
    }
    BAR();
    const int which = w & 1, tt = (w >> 1) & 1, kh = w >> 2;
    u32x4 gf[8], df[4];
#pragma unroll
    for (int s = 0; s < 8; ++s) gf[s] = tr_frag(smem, which * 32768 + (kh * 256 + 32 * s) * 64 + tt * 32, 64, g, n);
#pragma unroll
    for (int i = 0; i < 4; ++i) df[i] = tr_frag(smem, L_WD + (4 * w + i) * 32, WDSTR, g, n);
    if (w < 4) {
      for (u32 spin = 0; spin < SPIN_CAP; ++spin) {
        FENCE();
        const u32x2 sg = ld64<AUX_SC1>(rws, (lane & 31) * 128, WS_A + (pB + 56 * (w & 1)) * 4096);
        if (__all(gok(sg[0], sg[1], tag))) break;
        __builtin_amdgcn_s_sleep(2);
      }
      STAMP(11);
      u32x4 gh[4];
      bool ok[4] = {false, false, false, false};
      for (u32 spin = 0; spin < SPIN_CAP; ++spin) {
        FENCE();
#pragma unroll
        for (int i = 0; i < 4; ++i) if (!ok[i]) gh[i] = ld128<AUX_SC1>(rws, lane * 16 + i * 1024, WS_H + w * 4096);
#pragma unroll
        for (int i = 0; i < 4; ++i) ok[i] = ok[i] || gok2(gh[i], tag);
        if (__all(ok[0] && ok[1] && ok[2] && ok[3])) break;
      }
      STAMP(12);
      float hv[8];
#pragma unroll
      for (int i = 0; i < 4; ++i) { hv[2 * i] = gval(gh[i][0]); hv[2 * i + 1] = gval(gh[i][2]); }
      const float mean = sum_all(((hv[0] + hv[1]) + (hv[2] + hv[3])) + ((hv[4] + hv[5]) + (hv[6] + hv[7]))) * (1.f / 512);
      float v = 0.f;
#pragma unroll
      for (int i = 0; i < 8; ++i) { const float d = hv[i] - mean; v += d * d; }
      v = sum_all(v) * (1.f / 512);
      const float mb = rbf(mean), rden = 1.f / rbf(sqrtf(rbf(rbf(v) + 1e-5f)));
#pragma unroll
      for (int i = 0; i < 4; ++i) {
        const u32 d = cvtpk(hv[2 * i] - mb, hv[2 * i + 1] - mb);
        *(u32*)(smem + L_HN + w * 1088 + (lane + 64 * i) * 4) = cvtpk(bflo(d) * rden, bfhi(d) * rden);
      }
    }
    BAR();
    f32x4 ga = {0.f, 0.f, 0.f, 0.f};
#pragma unroll
    for (int s = 0; s < 8; ++s) ga = mfma16(gf[s], *(const u32x4*)(smem + L_HN + (n & 3) * 1088 + (kh * 256 + 32 * s + 8 * g) * 2), ga);
    if (n < 4) *(f32x4*)(smem + L_RED + (((kh * 2 + which) * 4 + n) * 32 + 16 * tt + 4 * g) * 4) = ga;
    BAR();
    if (tid < 128) {
      const float* red = (const float*)(smem + L_RED);
      const float gs = rbf(red[tid] + red[256 + tid]), us = rbf(red[128 + tid] + red[384 + tid]);
      const float sg = rbf(gs / (1.f + __builtin_amdgcn_exp2f(-gs * LOG2E)));
      *(unsigned short*)(smem + L_ACT + tid * 2) = (unsigned short)cvtpk(sg * us, 0.f);
    }
    BAR();
    const u32x4 actf = *(const u32x4*)(smem + L_ACT + (n & 3) * 64 + g * 16);
    f32x4 y[4];
#pragma unroll
    for (int i = 0; i < 4; ++i) y[i] = mfma16(df[i], actf, f32x4{0.f, 0.f, 0.f, 0.f});
    if (n < 4) {
#pragma unroll
      for (int i = 0; i < 4; ++i) {
        const int vo = n * 32 * 7168 + g * 32, so = WS_Y + (4 * w + i) * 7168 + pB * 128;
        st128_sc1(gpair(y[i][0], y[i][1], tag), rws, vo, so);
        st128_sc1(gpair(y[i][2], y[i][3], tag), rws, vo + 16, so);
      }
    }
    STAMP(13);
  }
}

extern "C" void kernel_launch(void* const* d_in, const int* in_sizes, int n_in, void* d_out, int out_size, void* d_ws, size_t ws_size,
                              hipStream_t stream) {
  Params p;
  p.x = d_in[0]; p.k = d_in[1]; p.v = d_in[2]; p.wq = d_in[3]; p.wo = d_in[4];
  p.wg = d_in[6]; p.wu = d_in[7]; p.wd = d_in[8];
  p.out = d_out; p.ws = d_ws;
  fused_block<<<NWG, NTHR, 0, stream>>>(p);
}
```

```cpp
#include <hip/hip_runtime.h>
#include <stdint.h>

typedef unsigned u32;
typedef unsigned long long u64;
typedef unsigned u32x2 __attribute__((ext_vector_type(2)));
typedef unsigned u32x4 __attribute__((ext_vector_type(4)));
typedef float f32x2 __attribute__((ext_vector_type(2)));
typedef float f32x4 __attribute__((ext_vector_type(4)));
typedef short bf16x8 __attribute__((ext_vector_type(8)));
typedef short s16x4 __attribute__((ext_vector_type(4)));
typedef __bf16 bf16x2_t __attribute__((ext_vector_type(2)));
typedef __amdgpu_buffer_rsrc_t rsrc_t;
#define LAS __attribute__((address_space(3)))

constexpr int NWG = 256, NTHR = 512;
constexpr int AUX_NT = 2, AUX_SC1 = 16;
#define PACE 32
#define NT_A 65
#define NT_Q 62
#define AB_NOW 0
#define AB_NOQ 0
#define AB_NOC 0
#define W_LATE 1
#define AB_NOPOLL 0
#define WQSEG 128
#define WQAUX 2
#define MAP2 1
#define POLL_T0 0

constexpr int WS_E = 0;
constexpr int WS_Q = 4096;
constexpr int PREC = 1152;
constexpr int WS_P = WS_Q + 4 * 4096 * 8;
constexpr int WS_A = WS_P + 128 * 8 * PREC;
constexpr int WS_H = WS_A + 128 * 32 * 128;
constexpr int WS_Y = WS_H + 2048 * 8;
constexpr int WS_END = WS_Y + 128 * 56 * 128;
#define STAMP(i) do { } while (0)
#define STAMP_DECL
#define STAMP_FLUSH() do { } while (0)

#define ESTAMP(i) do { } while (0)
constexpr int WOSTR = 288, WDSTR = 1056;
constexpr int L_W = 0;
constexpr int L_WG = 0, L_WU = 32768, L_WD = 65536;
constexpr int L_MRG = L_WD + 32 * WDSTR;
constexpr int L_QS = L_MRG + 8 * 4 * 132 * 4;
constexpr int L_OB = L_QS + 1280;
constexpr int L_HN = L_OB + 1280;
constexpr int L_RED = L_HN + 4 * 1088;
constexpr int L_ACT = L_RED + 2048;
constexpr int L_QRED = L_MRG;
constexpr int L_TAG = L_ACT + 256;
constexpr int LDS_BYTES = L_TAG + 16;

constexpr u32 SPIN_CAP = 1u << 16;

struct Params {
  const void* x; const void* k; const void* v; const void* wq; const void* wo; const void* wg; const void* wu; const void* wd;
  void* out; void* ws;
};

__device__ __forceinline__ float bflo(u32 w) { return __builtin_bit_cast(float, w << 16); }
__device__ __forceinline__ float bfhi(u32 w) { return __builtin_bit_cast(float, w & 0xffff0000u); }
__device__ __forceinline__ u32 cvtpk(float lo, float hi) {
  f32x2 v = {lo, hi};
  return __builtin_bit_cast(u32, __builtin_convertvector(v, bf16x2_t));
}
__device__ __forceinline__ float rbf(float x) { return bflo(cvtpk(x, x)); }
__device__ __forceinline__ f32x4 mfma16(u32x4 a, u32x4 b, f32x4 c) {
  return __builtin_amdgcn_mfma_f32_16x16x32_bf16(__builtin_bit_cast(bf16x8, a), __builtin_bit_cast(bf16x8, b), c, 0, 0, 0);
}
template <int CTRL> __device__ __forceinline__ float dpp(float x) {
  return __builtin_bit_cast(float, __builtin_amdgcn_mov_dpp(__builtin_bit_cast(int, x), CTRL, 0xf, 0xf, true));
}
constexpr int XOR1 = 0xB1, XOR2 = 0x4E, ROR4 = 0x124, ROR8 = 0x128;
__device__ __forceinline__ float x16_32_max(float x) {
  auto s = __builtin_amdgcn_permlane16_swap(__float_as_uint(x), __float_as_uint(x), false, false);
  x = fmaxf(__uint_as_float(s[0]), __uint_as_float(s[1]));
  auto t = __builtin_amdgcn_permlane32_swap(__float_as_uint(x), __float_as_uint(x), false, false);
  return fmaxf(__uint_as_float(t[0]), __uint_as_float(t[1]));
}
__device__ __forceinline__ float x16_32_sum(float x) {
  auto s = __builtin_amdgcn_permlane16_swap(__float_as_uint(x), __float_as_uint(x), false, false);
  x = __uint_as_float(s[0]) + __uint_as_float(s[1]);
  auto t = __builtin_amdgcn_permlane32_swap(__float_as_uint(x), __float_as_uint(x), false, false);
  return __uint_as_float(t[0]) + __uint_as_float(t[1]);
}
__device__ __forceinline__ float sum_b345(float x) { x += dpp<ROR8>(x); return x16_32_sum(x); }
__device__ __forceinline__ float max_b345(float x) { x = fmaxf(x, dpp<ROR8>(x)); return x16_32_max(x); }
__device__ __forceinline__ float sum_b2345(float x) { x += dpp<ROR4>(x); return sum_b345(x); }
__device__ __forceinline__ float sum_all(float x) { x += dpp<XOR1>(x); x += dpp<XOR2>(x); return sum_b2345(x); }
__device__ __forceinline__ float pick4(f32x4 v, int g) { const float a = (g & 1) ? v[1] : v[0], b = (g & 1) ? v[3] : v[2]; return (g & 2) ? b : a; }

__device__ __forceinline__ rsrc_t mk_rsrc(const void* p, u32 bytes) { return __builtin_amdgcn_make_buffer_rsrc((void*)p, 0, bytes, 0x00020000); }
template <int AUX> __device__ __forceinline__ u32x4 ld128(rsrc_t r, int vo, int so) { return __builtin_amdgcn_raw_buffer_load_b128(r, vo, so, AUX); }
template <int AUX> __device__ __forceinline__ u32 ld32(rsrc_t r, int vo, int so) { return __builtin_amdgcn_raw_buffer_load_b32(r, vo, so, AUX); }
__device__ __forceinline__ u32x4 ld128_sc1(rsrc_t r, int o) { return __builtin_amdgcn_raw_buffer_load_b128(r, o, 0, AUX_SC1); }
__device__ __forceinline__ u32x2 ld64_sc1(rsrc_t r, int o) { return __builtin_amdgcn_raw_buffer_load_b64(r, o, 0, AUX_SC1); }
__device__ __forceinline__ void st128_sc1(u32x4 v, rsrc_t r, int o) { __builtin_amdgcn_raw_buffer_store_b128(v, r, o, 0, AUX_SC1); }
__device__ __forceinline__ void st64_sc1(u32x2 v, rsrc_t r, int o) { __builtin_amdgcn_raw_buffer_store_b64(v, r, o, 0, AUX_SC1); }

#define BAR() asm volatile("s_waitcnt lgkmcnt(0)\n\ts_barrier" ::: "memory")
#define FENCE() asm volatile("" ::: "memory")
#define SCHED() __builtin_amdgcn_sched_barrier(0)

struct Tag { u32 lo, hi; };
__device__ __forceinline__ Tag mk_tag(u32x2 e) {
  const u64 e1 = (((u64)(u32)__builtin_amdgcn_readfirstlane(e[1]) << 32) | (u32)__builtin_amdgcn_readfirstlane(e[0])) + 1ull;
  return Tag{(u32)e1, (u32)(e1 >> 32) & 0xffu};
}
__device__ __forceinline__ u32 gpay(float v, Tag t) { return (__float_as_uint(v) & 0xffffff00u) | t.hi; }
__device__ __forceinline__ u32x2 gone(float a, Tag t) { return u32x2{gpay(a, t), t.lo}; }
__device__ __forceinline__ u32x4 gpair(float a, float b, Tag t) { return u32x4{gpay(a, t), t.lo, gpay(b, t), t.lo}; }
__device__ __forceinline__ float gval(u32 pay) { return __uint_as_float(pay & 0xffffff00u); }
template <int N> __device__ __forceinline__ u32 gbad(const u32x4 (&g)[N], Tag t) {
  u32 a = 0, b = 0;
#pragma unroll
  for (int i = 0; i < N; ++i) { a |= (g[i][1] ^ t.lo) | (g[i][3] ^ t.lo); b |= (g[i][0] ^ t.hi) | (g[i][2] ^ t.hi); }
  return a | (b & 0xffu);
}
template <int N> __device__ __forceinline__ void sweep(u32x4 (&g)[N], rsrc_t r, const int (&off)[N], Tag t) {
  for (u32 spin = 0; spin < SPIN_CAP; ++spin) {
    FENCE();
#pragma unroll
    for (int i = 0; i < N; ++i) g[i] = ld128_sc1(r, off[i]);
    if (__all(gbad<N>(g, t) == 0)) break;
  }
}

__device__ __forceinline__ void ln_finish(float mean, float var, float& mb, float& rden) {
  mb = rbf(mean);
  rden = __builtin_amdgcn_rcpf(rbf(__builtin_amdgcn_sqrtf(rbf(rbf(var) + 1e-5f))));
}
__device__ __forceinline__ u32 ln_pair(float a, float b, float mb, float rden) {
  const u32 d = cvtpk(a - mb, b - mb);
  return cvtpk(bflo(d) * rden, bfhi(d) * rden);
}

__device__ __forceinline__ u32x2 tr_read(char* smem, int off) {
  return __builtin_bit_cast(u32x2, __builtin_amdgcn_ds_read_tr16_b64_v4i16((LAS s16x4*)(smem + off)));
}
__device__ __forceinline__ u32x4 tr_frag(char* smem, int off, int str, int g, int n) {
  const int o = off + (8 * g + (n >> 2)) * str + (n & 3) * 8;
  const u32x2 a = tr_read(smem, o), b = tr_read(smem, o + 4 * str);
  return u32x4{a[0], a[1], b[0], b[1]};
}

__global__ void __launch_bounds__(NTHR, 2) fused_block(Params p) {
  __shared__ __attribute__((aligned(16))) char smem[LDS_BYTES];
  const int c = blockIdx.x, tid = threadIdx.x, lane = tid & 63, w = __builtin_amdgcn_readfirstlane(tid >> 6);
  const int g = lane >> 4, n = lane & 15;
  const int grp = c >> 3, split = (c + grp) & 7;
  const int pB = 7 * (c & 7) + (grp >> 2);
  const bool roleB = (split & 3) == 3 && (grp >> 2) < 7;
  const int b = grp >> 3, kvh = grp & 7;
  const bool roleA = (split & 1) == 0, roleQ = (split & 3) == 1;
  const int ridx = grp * 4 + (split >> 1);
  constexpr int NT_B = 256 - 2 * NT_A - NT_Q;
  const int ntiles = roleA ? NT_A : roleQ ? NT_Q : NT_B;
  const int tile0 = 256 * (split >> 2) + ((split & 3) > 0 ? NT_A : 0) + ((split & 3) > 1 ? NT_Q : 0) + ((split & 3) > 2 ? NT_A : 0);
  STAMP_DECL;
  STAMP(0);

  const rsrc_t rws = mk_rsrc(p.ws, WS_END);
  const rsrc_t rx = mk_rsrc(p.x, 4096);
  const rsrc_t rk = mk_rsrc(p.k, 128u << 20);
  const rsrc_t rv = mk_rsrc(p.v, 128u << 20);

  const u64 tstart = POLL_T0 ? __builtin_amdgcn_s_memrealtime() : 0;
  u32x2 eraw = {0u, 0u};
  if (w == 0) eraw = ld64_sc1(rws, WS_E);
  const int kvo = (4 * (n & 3) + (n >> 2)) * 256 + g * 16, vvo = lane * 16;
  const int sbase = grp * 16384 * 256 + tile0 * 8192;
  u32x4 K0[8], V0[8], K1[8], V1[8];
  auto tile_so = [&](int tile) { int so = __builtin_amdgcn_readfirstlane(sbase + tile * 8192); asm volatile("" : "+s"(so)); return so; };
  auto ld1K = [&](int j, int so) { return ld128<AUX_NT>(rk, kvo + (j & 3) * 64, so + (j >> 2) * 4096); };
  auto ld1V = [&](int j, int so) { return ld128<AUX_NT>(rv, vvo + (j & 3) * 1024, so + (j >> 2) * 4096); };
  auto ldKV = [&](u32x4 (&kk)[8], u32x4 (&vv)[8], int so, u32 bytes) {
    const rsrc_t rk2 = mk_rsrc(p.k, bytes), rv2 = mk_rsrc(p.v, bytes);
#pragma unroll
    for (int j = 0; j < 8; ++j) { kk[j] = ld128<AUX_NT>(rk2, kvo + (j & 3) * 64, so + (j >> 2) * 4096); SCHED(); }
#pragma unroll
    for (int j = 0; j < 8; ++j) { vv[j] = ld128<AUX_NT>(rv2, vvo + (j & 3) * 1024, so + (j >> 2) * 4096); SCHED(); }
  };
#define PACED() do { asm volatile("s_waitcnt vmcnt(%0)" :: "i"(PACE - 1) : "memory"); SCHED(); } while (0)

  Tag tag;
  auto lds_tag = [&]() { const u32x2 t = *(const u32x2*)(smem + L_TAG); return Tag{(u32)__builtin_amdgcn_readfirstlane(t[0]), (u32)__builtin_amdgcn_readfirstlane(t[1])}; };
  const int col0 = kvh * 512 + (2 * b + (split >> 2)) * 64;
  u32x4 xr, wqr[8];
  if (roleQ && !AB_NOQ) {
    xr = ld128<0>(rx, lane * 16, (w & 3) * 1024);
    const rsrc_t rwq = mk_rsrc(p.wq, 4u << 20);
#pragma unroll
    for (int i = 0; i < 8; ++i) {
      wqr[i] = ld128<WQAUX>(rwq, (lane >> 3) * 8192 + (lane & 7) * 16, (64 * w + 8 * i) * 8192 + col0 * 2);
    }
    FENCE(); SCHED();
    BAR();
  }
  const int so0 = tile_so(w);
#pragma unroll
  for (int j = 0; j < 4; ++j) { K0[j] = ld1K(j, so0); SCHED(); }
  FENCE(); SCHED();
  if (roleQ && !AB_NOQ) {
    if (w < 4) {
      float hv[8];
#pragma unroll
      for (int i = 0; i < 4; ++i) { hv[2 * i] = bflo(xr[i]); hv[2 * i + 1] = bfhi(xr[i]); }
      const float mean = sum_all(((hv[0] + hv[1]) + (hv[2] + hv[3])) + ((hv[4] + hv[5]) + (hv[6] + hv[7]))) * (1.f / 512);
      float v = 0.f;
#pragma unroll
      for (int i = 0; i < 8; ++i) { const float d = hv[i] - mean; v += d * d; }
      float mb, rden;
      ln_finish(mean, sum_all(v) * (1.f / 512), mb, rden);
      *(u32x4*)(smem + L_HN + w * 1088 + lane * 16) = u32x4{ln_pair(hv[0], hv[1], mb, rden), ln_pair(hv[2], hv[3], mb, rden), ln_pair(hv[4], hv[5], mb, rden), ln_pair(hv[6], hv[7], mb, rden)};
    }
    STAMP(15);
    if (w == 0) { tag = mk_tag(eraw); *(u32x2*)(smem + L_TAG) = u32x2{tag.lo, tag.hi}; }
    char* wl = smem + L_W + w * 8192;
#pragma unroll
    for (int i = 0; i < 8; ++i) *(u32x4*)(wl + i * 1024 + lane * 16) = wqr[i];
    STAMP(1);
    u32x4 wf[8];
#pragma unroll
    for (int i = 0; i < 8; ++i) wf[i] = tr_frag(wl, (i >> 2) * 4096 + (i & 3) * 32, 128, g, n);
    BAR();
    const u32x4 x0 = *(const u32x4*)(smem + L_HN + (n & 3) * 1088 + (64 * w + 8 * g) * 2), x1 = *(const u32x4*)(smem + L_HN + (n & 3) * 1088 + (64 * w + 32 + 8 * g) * 2);
#pragma unroll
    for (int t = 0; t < 4; ++t) {
      const f32x4 qa = mfma16(x1, wf[4 + t], mfma16(x0, wf[t], f32x4{0.f, 0.f, 0.f, 0.f}));
      *(float*)(smem + L_QRED + ((w * 4 + g) * 64 + 16 * t + n) * 4) = pick4(qa, g);
    }
    BAR();
    if (w < 4) {
      tag = lds_tag();
      float q = 0.f;
#pragma unroll
      for (int ww = 0; ww < 8; ++ww) q += *(const float*)(smem + L_QRED + ((ww * 4 + w) * 64 + lane) * 4);
      st64_sc1(gone(rbf(q), tag), rws, WS_Q + (w * 4096 + col0 + lane) * 8);
    }
    BAR();
    STAMP(2);
  }
  FENCE(); SCHED();
#pragma unroll
  for (int j = 4; j < 8; ++j) { if (j >= PACE) PACED(); K0[j] = ld1K(j, so0); SCHED(); }
#pragma unroll
  for (int j = 0; j < 8; ++j) { PACED(); V0[j] = ld1V(j, so0); SCHED(); }
  if (w == 0) {
    STAMP(3);
    tag = mk_tag(eraw);
    *(u32x2*)(smem + L_TAG) = u32x2{tag.lo, tag.hi};
    if (POLL_T0) while (__builtin_amdgcn_s_memrealtime() - tstart < POLL_T0) __builtin_amdgcn_s_sleep(2);
    u32x4 gq[4];
    int off[4];
#pragma unroll
    for (int i = 0; i < 4; ++i) off[i] = WS_Q + (b * 4096 + kvh * 512) * 8 + i * 1024 + lane * 16;
    if (AB_NOQ || AB_NOPOLL) { for (int i = 0; i < 4; ++i) gq[i] = u32x4{0x3c003c00u, 0, 0x3c003c00u, 0}; } else sweep<4>(gq, rws, off, tag);
#pragma unroll
    for (int i = 0; i < 4; ++i) *(u32*)(smem + L_QS + i * 320 + lane * 4) = __builtin_amdgcn_perm(gq[i][2], gq[i][0], 0x07060302u);
    STAMP(4);
    BAR();
  }
  FENCE(); SCHED();
  {
    const int so = tile_so(8 + w);
#pragma unroll
    for (int j = 0; j < 8; ++j) { PACED(); K1[j] = ld1K(j, so); SCHED(); }
#pragma unroll
    for (int j = 0; j < 8; ++j) { PACED(); V1[j] = ld1V(j, so); SCHED(); }
  }
  FENCE(); SCHED();
  if (w != 0) { STAMP(3); BAR(); }
  tag = lds_tag();
  u32x4 qf[4];
#pragma unroll
  for (int s = 0; s < 4; ++s) qf[s] = *(const u32x4*)(smem + L_QS + (n & 3) * 320 + s * 64 + g * 16);
  STAMP(5);

  constexpr float SCALE = 0.08838834764831845f, LOG2E = 1.4426950408889634f;
  float m = -INFINITY, l = 0.f;
  f32x4 acc[8];
#pragma unroll
  for (int e = 0; e < 8; ++e) acc[e] = f32x4{0.f, 0.f, 0.f, 0.f};
  auto step = [&](u32x4 (&kk)[8], u32x4 (&vv)[8], bool more, int next, u32 bytes) {
    f32x4 s0 = {0.f, 0.f, 0.f, 0.f}, s1 = {0.f, 0.f, 0.f, 0.f};
#pragma unroll
    for (int s = 0; s < 4; ++s) { s0 = mfma16(kk[s], qf[s], s0); s1 = mfma16(kk[4 + s], qf[s], s1); }
    float t[8];
#pragma unroll
    for (int r = 0; r < 4; r += 2) {
      const u32 a0 = cvtpk(s0[r], s0[r + 1]), a1 = cvtpk(s1[r], s1[r + 1]);
      const u32 b0 = cvtpk(bflo(a0) * SCALE, bfhi(a0) * SCALE), b1 = cvtpk(bflo(a1) * SCALE, bfhi(a1) * SCALE);
      t[r] = bflo(b0); t[r + 1] = bfhi(b0); t[4 + r] = bflo(b1); t[5 + r] = bfhi(b1);
    }
    float tm = fmaxf(fmaxf(fmaxf(t[0], t[1]), fmaxf(t[2], t[3])), fmaxf(fmaxf(t[4], t[5]), fmaxf(t[6], t[7])));
    tm = x16_32_max(tm);
    if (__any(tm > m)) {
      const float mn = fmaxf(m, tm);
      const float alpha = __builtin_amdgcn_exp2f((m - mn) * LOG2E);
      l *= alpha;
#pragma unroll
      for (int e = 0; e < 8; ++e) acc[e] *= alpha;
      m = mn;
    }
    float pr[8];
#pragma unroll
    for (int r = 0; r < 8; ++r) { pr[r] = __builtin_amdgcn_exp2f((t[r] - m) * LOG2E); l += pr[r]; }
    const u32x4 pf = {cvtpk(pr[0], pr[1]), cvtpk(pr[2], pr[3]), cvtpk(pr[4], pr[5]), cvtpk(pr[6], pr[7])};
#pragma unroll
    for (int i = 0; i < 4; ++i) {
      u32x4 flo, fhi;
#pragma unroll
      for (int q = 0; q < 4; ++q) {
        flo[q] = __builtin_amdgcn_perm(vv[2 * q + 1][i], vv[2 * q][i], 0x05040100u);
        fhi[q] = __builtin_amdgcn_perm(vv[2 * q + 1][i], vv[2 * q][i], 0x07060302u);
      }
      acc[2 * i] = mfma16(flo, pf, acc[2 * i]);
      acc[2 * i + 1] = mfma16(fhi, pf, acc[2 * i + 1]);
    }
    SCHED();
    if (more) ldKV(kk, vv, next, bytes);
    SCHED();
  };
  {
    const int young = ntiles >> 3, left = ntiles - 8 * young;
    const int cnt = young + (w < 4 ? (left + 3 - w) >> 2 : 0);
    auto tid_of = [&](int i) { return i < young ? w + 8 * i : 8 * young + 4 * (i - young) + w; };
#pragma unroll 1
    for (int i = 2; i < cnt; i += 2) {
      step(K0, V0, true, tile_so(tid_of(i)), 128u << 20);
      step(K1, V1, true, tile_so(tid_of(i + 1 < cnt ? i + 1 : i)), i + 1 < cnt ? 128u << 20 : 0u);
    }
    step(K0, V0, false, 0, 0);
    if (!(cnt & 1)) step(K1, V1, false, 0, 0);
  }
  const int head = ridx >> 2, quarter = ridx & 3;
  u32x4 wr[12];
  auto ld_w = [&]() {
    const rsrc_t rw = mk_rsrc(roleA ? p.wo : p.wd, AB_NOW ? 0u : roleA ? (4u << 20) : roleB ? 1792u * 512 * 2 : 0u);
    const int vo = roleA ? g * 1024 + n * 16 : lane * 16;
    const int so = roleA ? (head * 128 + 16 * w) * 1024 + quarter * 256 : (32 * pB + 4 * w) * 1024, st = roleA ? 4096 : 1024;
#pragma unroll
    for (int i = 0; i < 4; ++i) wr[8 + i] = ld128<AUX_NT>(rw, vo, so + i * st);
  };
  if (!W_LATE) ld_w();
  SCHED();

  l = x16_32_sum(l);
  {
    float* mrg = (float*)(smem + L_MRG);
    if (n < 4) {
      float* rec = mrg + (w * 4 + n) * 132;
#pragma unroll
      for (int r = 0; r < 4; ++r) {
        *(f32x4*)(rec + 8 * (4 * g + r)) = f32x4{acc[0][r], acc[1][r], acc[2][r], acc[3][r]};
        *(f32x4*)(rec + 8 * (4 * g + r) + 4) = f32x4{acc[4][r], acc[5][r], acc[6][r], acc[7][r]};
      }
      if (g == 0) *(f32x2*)(rec + 128) = f32x2{m, l};
    }
    BAR();
    STAMP(6);
    if (w < 4) {
      f32x2 ml[8], ov[8];
#pragma unroll
      for (int ww = 0; ww < 8; ++ww) { ml[ww] = *(const f32x2*)(mrg + (ww * 4 + w) * 132 + 128); ov[ww] = *(const f32x2*)(mrg + (ww * 4 + w) * 132 + 2 * lane); }
      float M = ml[0][0];
#pragma unroll
      for (int ww = 1; ww < 8; ++ww) M = fmaxf(M, ml[ww][0]);
      float L = 0.f, o0 = 0.f, o1 = 0.f;
#pragma unroll
      for (int ww = 0; ww < 8; ++ww) {
        const float wt = __builtin_amdgcn_exp2f((ml[ww][0] - M) * LOG2E);
        L += wt * ml[ww][1]; o0 += wt * ov[ww][0]; o1 += wt * ov[ww][1];
      }
      const int so = WS_P + ((b * 32 + kvh * 4 + w) * 8 + split) * PREC;
      st128_sc1(gpair(o0, o1, tag), rws, so + lane * 16);
      if (lane < 8) st128_sc1(gpair(M, L, tag), rws, so + 1024 + lane * 16);
    }
  }
  STAMP(7);
  if (W_LATE) { if (W_LATE == 2) __builtin_amdgcn_s_barrier(); ld_w(); }

  if (roleA) {
    const rsrc_t rout = mk_rsrc(p.out, 4096);
    const int bR = ridx >> 5, tR = ridx & 31, hg = lane >> 3, j = lane & 7;
    const u32 xres = ld32<0>(rx, (bR * 512 + 16 * tR + 2 * j) * 2, 0);
#pragma unroll
    for (int i = 0; i < 4; ++i) *(u32x4*)(smem + L_W + (16 * w + 4 * i + g) * WOSTR + n * 16) = wr[8 + i];
    BAR();
    u32x4 af[4];
#pragma unroll
    for (int s = 0; s < 4; ++s) af[s] = tr_frag(smem, L_W + 32 * s * WOSTR + w * 32, WOSTR, g, n);
    {
      const int bA = w >> 1, dh = w & 1;
      const int o = WS_P + ((bA * 32 + head) * 8 + hg) * PREC + j * 16;
      u32x4 gv[5] = {{0, 0, 0, 0}, {0, 0, 0, 0}, {0, 0, 0, 0}, {0, 0, 0, 0}, {0, 0, 0, 0}};
      bool todo = true;
      for (u32 spin = 0; spin < SPIN_CAP; ++spin) {
        FENCE();
        if (todo) {
          gv[4] = ld128_sc1(rws, o + 1024);
#pragma unroll
          for (int i = 0; i < 4; ++i) gv[i] = ld128_sc1(rws, o + dh * 512 + i * 128);
        }
        todo = gbad<5>(gv, tag) != 0;
        const int missing = __builtin_popcountll(__ballot(todo));
        if (missing == 0) break;
        if (missing > 16) __builtin_amdgcn_s_sleep(8);
      }
      STAMP(8);
      const float ms = gval(gv[4][0]), ls = gval(gv[4][2]);
      const float M = max_b345(ms);
      const float wt = __builtin_amdgcn_exp2f((ms - M) * LOG2E);
      const float inv = __builtin_amdgcn_rcpf(sum_b345(wt * ls));
      u32 ob[4];
#pragma unroll
      for (int i = 0; i < 4; ++i) ob[i] = cvtpk(sum_b345(wt * gval(gv[i][0])) * inv, sum_b345(wt * gval(gv[i][2])) * inv);
      if (hg == 0) {
#pragma unroll
        for (int i = 0; i < 4; ++i) *(u32*)(smem + L_OB + bA * 320 + (dh * 64 + 16 * i + 2 * j) * 2) = ob[i];
      }
    }
    BAR();
    f32x4 aa = {0.f, 0.f, 0.f, 0.f};
#pragma unroll
    for (int s = 0; s < 4; ++s) aa = mfma16(*(const u32x4*)(smem + L_OB + (n & 3) * 320 + s * 64 + g * 16), af[s], aa);
    st64_sc1(gone(pick4(aa, g), tag), rws, WS_A + (g * 32 + quarter * 8 + w) * 4096 + head * 128 + n * 8);
    STAMP(9);
    if (w != 0) { STAMP_FLUSH(); return; }
    float h0, h1;
    {
      u32x4 ga[4];
      int off[4];
#pragma unroll
      for (int i = 0; i < 4; ++i) off[i] = WS_A + ridx * 4096 + i * 1024 + lane * 16;
      sweep<4>(ga, rws, off, tag);
      STAMP(10);
      const float a0 = sum_b345((gval(ga[0][0]) + gval(ga[1][0])) + (gval(ga[2][0]) + gval(ga[3][0])));
      const float a1 = sum_b345((gval(ga[0][2]) + gval(ga[1][2])) + (gval(ga[2][2]) + gval(ga[3][2])));
      h0 = rbf(bflo(xres) + rbf(a0)); h1 = rbf(bfhi(xres) + rbf(a1));
      if (hg == 0) st128_sc1(gpair(h0, h1, tag), rws, WS_H + (bR * 512 + 16 * tR) * 8 + j * 16);
    }
    {
      u32x4 gy[7];
      int off[7];
#pragma unroll
      for (int i = 0; i < 7; ++i) off[i] = WS_Y + ridx * 7168 + i * 1024 + lane * 16;
      sweep<7>(gy, rws, off, tag);
      STAMP(14);
      float y0 = 0.f, y1 = 0.f;
#pragma unroll
      for (int i = 0; i < 7; ++i) { y0 += gval(gy[i][0]); y1 += gval(gy[i][2]); }
      y0 = sum_b345(y0); y1 = sum_b345(y1);
      if (hg == 0) __builtin_amdgcn_raw_buffer_store_b32(cvtpk(h0 + rbf(y0), h1 + rbf(y1)), rout, (bR * 512 + 16 * tR + 2 * j) * 2, 0, 0);
      if (ridx == 0 && lane == 0) {
        const u64 e1 = (((u64)eraw[1] << 32) | eraw[0]) + 1ull;
        st64_sc1(u32x2{(u32)e1, (u32)(e1 >> 32)}, rws, WS_E);
      }
    }
  } else if (roleB) {
    {
      const rsrc_t rwg = mk_rsrc(p.wg, AB_NOW ? 0 : 512 * 1792 * 2), rwu = mk_rsrc(p.wu, AB_NOW ? 0 : 512 * 1792 * 2);
#pragma unroll
      for (int i = 0; i < 4; ++i) wr[i] = ld128<AUX_NT>(rwg, (lane >> 2) * 3584 + (lane & 3) * 16, (64 * w + 16 * i) * 3584 + pB * 64);
#pragma unroll
      for (int i = 0; i < 4; ++i) wr[4 + i] = ld128<AUX_NT>(rwu, (lane >> 2) * 3584 + (lane & 3) * 16, (64 * w + 16 * i) * 3584 + pB * 64);
#pragma unroll
      for (int i = 0; i < 4; ++i) {
        *(u32x4*)(smem + L_WG + (4 * w + i) * 1024 + lane * 16) = wr[i];
        *(u32x4*)(smem + L_WU + (4 * w + i) * 1024 + lane * 16) = wr[4 + i];
        *(u32x4*)(smem + L_WD + (4 * w + i) * WDSTR + lane * 16) = wr[8 + i];
      }
    }
    BAR();
    const int which = w & 1, tt = (w >> 1) & 1, kh = w >> 2;
    u32x4 gf[8], df[4];
#pragma unroll
    for (int s = 0; s < 8; ++s) gf[s] = tr_frag(smem, which * 32768 + (kh * 256 + 32 * s) * 64 + tt * 32, 64, g, n);
#pragma unroll
    for (int i = 0; i < 4; ++i) df[i] = tr_frag(smem, L_WD + (4 * w + i) * 32, WDSTR, g, n);
    if (w < 4) {
#pragma unroll 1
      for (u32 spin = 0; spin < SPIN_CAP; ++spin) {
        FENCE();
        const u32x2 sg = ld64_sc1(rws, WS_A + (pB + 56 * (w & 1)) * 4096 + (lane & 31) * 128);
        if (__all(sg[1] == tag.lo)) break;
        __builtin_amdgcn_s_sleep(2);
      }
      STAMP(11);
      u32x4 gh[4];
      int off[4];
#pragma unroll
      for (int i = 0; i < 4; ++i) off[i] = WS_H + w * 4096 + i * 1024 + lane * 16;
      sweep<4>(gh, rws, off, tag);
      STAMP(12);
      float hv[8];
#pragma unroll
      for (int i = 0; i < 4; ++i) { hv[2 * i] = gval(gh[i][0]); hv[2 * i + 1] = gval(gh[i][2]); }
      const float mean = sum_all(((hv[0] + hv[1]) + (hv[2] + hv[3])) + ((hv[4] + hv[5]) + (hv[6] + hv[7]))) * (1.f / 512);
      float v = 0.f;
#pragma unroll
      for (int i = 0; i < 8; ++i) { const float d = hv[i] - mean; v += d * d; }
      float mb, rden;
      ln_finish(mean, sum_all(v) * (1.f / 512), mb, rden);
#pragma unroll
      for (int i = 0; i < 4; ++i) *(u32*)(smem + L_HN + w * 1088 + (lane + 64 * i) * 4) = ln_pair(hv[2 * i], hv[2 * i + 1], mb, rden);
    }
    BAR();
    f32x4 ga = {0.f, 0.f, 0.f, 0.f};
#pragma unroll
    for (int s = 0; s < 8; ++s) ga = mfma16(gf[s], *(const u32x4*)(smem + L_HN + (n & 3) * 1088 + (kh * 256 + 32 * s + 8 * g) * 2), ga);
    if (n < 4) *(f32x4*)(smem + L_RED + (((kh * 2 + which) * 4 + n) * 32 + 16 * tt + 4 * g) * 4) = ga;
    BAR();
    if (tid < 128) {
      const float* red = (const float*)(smem + L_RED);
      const float gs = rbf(red[tid] + red[256 + tid]), us = rbf(red[128 + tid] + red[384 + tid]);
      const float sg = rbf(gs * __builtin_amdgcn_rcpf(1.f + __builtin_amdgcn_exp2f(-gs * LOG2E)));
      *(unsigned short*)(smem + L_ACT + tid * 2) = (unsigned short)cvtpk(sg * us, 0.f);
    }
    BAR();
    const u32x4 actf = *(const u32x4*)(smem + L_ACT + (n & 3) * 64 + g * 16);
#pragma unroll
    for (int i = 0; i < 4; ++i) {
      const f32x4 y = mfma16(actf, df[i], f32x4{0.f, 0.f, 0.f, 0.f});
      st64_sc1(gone(pick4(y, g), tag), rws, WS_Y + (g * 32 + 4 * w + i) * 7168 + pB * 128 + n * 8);
    }
    STAMP(13);
  }
  STAMP_FLUSH();
}

extern "C" void kernel_launch(void* const* d_in, const int* in_sizes, int n_in, void* d_out, int out_size, void* d_ws, size_t ws_size,
                              hipStream_t stream) {
  Params p;
  p.x = d_in[0]; p.k = d_in[1]; p.v = d_in[2]; p.wq = d_in[3]; p.wo = d_in[4];
  p.wg = d_in[6]; p.wu = d_in[7]; p.wd = d_in[8];
  p.out = d_out; p.ws = d_ws;
  fused_block<<<NWG, NTHR, 0, stream>>>(p);
}
```

```cpp
#include <hip/hip_runtime.h>
#include <stdint.h>

typedef unsigned u32;
typedef unsigned long long u64;
typedef unsigned u32x2 __attribute__((ext_vector_type(2)));
typedef unsigned u32x4 __attribute__((ext_vector_type(4)));
typedef float f32x2 __attribute__((ext_vector_type(2)));
typedef float f32x4 __attribute__((ext_vector_type(4)));
typedef short bf16x8 __attribute__((ext_vector_type(8)));
typedef short s16x4 __attribute__((ext_vector_type(4)));
typedef __bf16 bf16x2_t __attribute__((ext_vector_type(2)));
typedef __amdgpu_buffer_rsrc_t rsrc_t;
#define LAS __attribute__((address_space(3)))

constexpr int NWG = 256, NTHR = 512;
constexpr int AUX_NT = 2, AUX_SC1 = 16;
#define PACE 32
#define NT_A 65
#define NT_Q 62
#define AB_NOW 0
#define AB_NOQ 0
#define AB_NOC 0
#define W_LATE 1
#define AB_NOPOLL 0
#define WQSEG 128
#define WQAUX 2
#define A_SLP 8
#define R_SLP 4
#define B_SLP 4
#define R2_DLY 80
#define H_DLY 0
#define TORD 2
#define B_BAR 1
#define MAP2 1
#define POLL_T0 0

constexpr int WS_E = 0;
constexpr int WS_Q = 4096;
constexpr int PREC = 1152;
constexpr int WS_P = WS_Q + 4 * 4096 * 8;
constexpr int WS_A = WS_P + 128 * 8 * PREC;
constexpr int WS_H = WS_A + 128 * 32 * 128;
constexpr int WS_Y = WS_H + 2048 * 8;
constexpr int WS_END = WS_Y + 128 * 56 * 128;
#define STAMP(i) do { } while (0)
#define STAMP_DECL
#define STAMP_FLUSH() do { } while (0)

#define ESTAMP(i) do { } while (0)
constexpr int WOSTR = 288, WDSTR = 1056;
constexpr int L_W = 0;
constexpr int L_WG = 0, L_WU = 32768, L_WD = 65536;
constexpr int L_MRG = L_WD + 32 * WDSTR;
constexpr int L_ML = L_MRG + 8 * 4 * 132 * 4;
constexpr int L_QS = L_ML + 256;
constexpr int L_OB = L_QS + 1280;
constexpr int L_HN = L_OB + 1280;
constexpr int L_ACT = L_HN + 4 * 1088;
constexpr int L_QRED = L_MRG;
constexpr int L_TAG = L_ACT + 256;
constexpr int LDS_BYTES = L_TAG + 16;

constexpr u32 SPIN_CAP = 1u << 16;

struct Params {
  const void* x; const void* k; const void* v; const void* wq; const void* wo; const void* wg; const void* wu; const void* wd;
  void* out; void* ws;
};

__device__ __forceinline__ float bflo(u32 w) { return __builtin_bit_cast(float, w << 16); }
__device__ __forceinline__ float bfhi(u32 w) { return __builtin_bit_cast(float, w & 0xffff0000u); }
__device__ __forceinline__ u32 cvtpk(float lo, float hi) {
  f32x2 v = {lo, hi};
  return __builtin_bit_cast(u32, __builtin_convertvector(v, bf16x2_t));
}
__device__ __forceinline__ float rbf(float x) { return bflo(cvtpk(x, x)); }
__device__ __forceinline__ f32x4 mfma16(u32x4 a, u32x4 b, f32x4 c) {
  return __builtin_amdgcn_mfma_f32_16x16x32_bf16(__builtin_bit_cast(bf16x8, a), __builtin_bit_cast(bf16x8, b), c, 0, 0, 0);
}
template <int CTRL> __device__ __forceinline__ float dpp(float x) {
  return __builtin_bit_cast(float, __builtin_amdgcn_mov_dpp(__builtin_bit_cast(int, x), CTRL, 0xf, 0xf, true));
}
constexpr int XOR1 = 0xB1, XOR2 = 0x4E, ROR4 = 0x124, ROR8 = 0x128;
__device__ __forceinline__ float vmax(float a, float b) { return __builtin_elementwise_maximum(a, b); }
__device__ __forceinline__ float x16_32_max(float x) {
  auto s = __builtin_amdgcn_permlane16_swap(__float_as_uint(x), __float_as_uint(x), false, false);
  x = vmax(__uint_as_float(s[0]), __uint_as_float(s[1]));
  auto t = __builtin_amdgcn_permlane32_swap(__float_as_uint(x), __float_as_uint(x), false, false);
  return vmax(__uint_as_float(t[0]), __uint_as_float(t[1]));
}
__device__ __forceinline__ float rs32(float a, float b) {
  auto s = __builtin_amdgcn_permlane32_swap(__float_as_uint(a), __float_as_uint(b), false, false);
  return __uint_as_float(s[0]) + __uint_as_float(s[1]);
}
__device__ __forceinline__ float rs16(float a, float b) {
  auto s = __builtin_amdgcn_permlane16_swap(__float_as_uint(a), __float_as_uint(b), false, false);
  return __uint_as_float(s[0]) + __uint_as_float(s[1]);
}
__device__ __forceinline__ float x16_32_sum(float x) {
  auto s = __builtin_amdgcn_permlane16_swap(__float_as_uint(x), __float_as_uint(x), false, false);
  x = __uint_as_float(s[0]) + __uint_as_float(s[1]);
  auto t = __builtin_amdgcn_permlane32_swap(__float_as_uint(x), __float_as_uint(x), false, false);
  return __uint_as_float(t[0]) + __uint_as_float(t[1]);
}
__device__ __forceinline__ float sum_b345(float x) { x += dpp<ROR8>(x); return x16_32_sum(x); }
__device__ __forceinline__ float max_b345(float x) { x = vmax(x, dpp<ROR8>(x)); return x16_32_max(x); }
__device__ __forceinline__ float sum_b2345(float x) { x += dpp<ROR4>(x); return sum_b345(x); }
__device__ __forceinline__ float sum_all(float x) { x += dpp<XOR1>(x); x += dpp<XOR2>(x); return sum_b2345(x); }
__device__ __forceinline__ float pick4(f32x4 v, int g) { const float a = (g & 1) ? v[1] : v[0], b = (g & 1) ? v[3] : v[2]; return (g & 2) ? b : a; }

__device__ __forceinline__ rsrc_t mk_rsrc(const void* p, u32 bytes) { return __builtin_amdgcn_make_buffer_rsrc((void*)p, 0, bytes, 0x00020000); }
template <int AUX> __device__ __forceinline__ u32x4 ld128(rsrc_t r, int vo, int so) { return __builtin_amdgcn_raw_buffer_load_b128(r, vo, so, AUX); }
template <int AUX> __device__ __forceinline__ u32 ld32(rsrc_t r, int vo, int so) { return __builtin_amdgcn_raw_buffer_load_b32(r, vo, so, AUX); }
__device__ __forceinline__ u32x4 ld128_sc1(rsrc_t r, int o) { return __builtin_amdgcn_raw_buffer_load_b128(r, o, 0, AUX_SC1); }
__device__ __forceinline__ u32x2 ld64_sc1(rsrc_t r, int o) { return __builtin_amdgcn_raw_buffer_load_b64(r, o, 0, AUX_SC1); }
__device__ __forceinline__ void st128_sc1(u32x4 v, rsrc_t r, int o) { __builtin_amdgcn_raw_buffer_store_b128(v, r, o, 0, AUX_SC1); }
__device__ __forceinline__ void st64_sc1(u32x2 v, rsrc_t r, int o) { __builtin_amdgcn_raw_buffer_store_b64(v, r, o, 0, AUX_SC1); }

#define BAR() asm volatile("s_waitcnt lgkmcnt(0)\n\ts_barrier" ::: "memory")
#define FENCE() asm volatile("" ::: "memory")
#define SCHED() __builtin_amdgcn_sched_barrier(0)

struct Tag { u32 lo, hi; };
__device__ __forceinline__ Tag mk_tag(u32x2 e) {
  const u64 e1 = (((u64)(u32)__builtin_amdgcn_readfirstlane(e[1]) << 32) | (u32)__builtin_amdgcn_readfirstlane(e[0])) + 1ull;
  return Tag{(u32)e1, (u32)(e1 >> 32) & 0xffu};
}
__device__ __forceinline__ u32 gpay(float v, Tag t) { return (__float_as_uint(v) & 0xffffff00u) | t.hi; }
__device__ __forceinline__ u32x2 gone(float a, Tag t) { return u32x2{gpay(a, t), t.lo}; }
__device__ __forceinline__ u32x4 gpair(float a, float b, Tag t) { return u32x4{gpay(a, t), t.lo, gpay(b, t), t.lo}; }
__device__ __forceinline__ float gval(u32 pay) { return __uint_as_float(pay & 0xffffff00u); }
template <int N> __device__ __forceinline__ u32 gbad32(const u32x4 (&g)[N], Tag t) {
  u32 a = g[0][1] ^ t.lo, b = g[0][3] ^ t.lo;
#pragma unroll
  for (int i = 1; i < N; ++i) { a = __builtin_amdgcn_bitop3_b32(a, g[i][1], t.lo, 0xF6); b = __builtin_amdgcn_bitop3_b32(b, g[i][3], t.lo, 0xF6); }
  return a | b;
}
template <int N> __device__ __forceinline__ u32 gbad8(const u32x4 (&g)[N], Tag t) {
  u32 a = g[0][0] ^ t.hi, b = g[0][2] ^ t.hi;
#pragma unroll
  for (int i = 1; i < N; ++i) { a = __builtin_amdgcn_bitop3_b32(a, g[i][0], t.hi, 0xF6); b = __builtin_amdgcn_bitop3_b32(b, g[i][2], t.hi, 0xF6); }
  return (a | b) & 0xffu;
}
__device__ __forceinline__ bool none(bool p) { return __builtin_amdgcn_ballot_w64(p) == 0; }
#define PINV(x) asm volatile("" : "+v"(x))
template <int N, int STR> __device__ __forceinline__ void sweep(u32x4 (&g)[N], rsrc_t r, int o, Tag t) {
#pragma unroll 1
  for (u32 spin = 0; spin < SPIN_CAP; ++spin) {
    FENCE();
#pragma unroll
    for (int i = 0; i < N; ++i) g[i] = ld128_sc1(r, o + i * STR);
    if (none((gbad32<N>(g, t) | gbad8<N>(g, t)) != 0)) break;
  }
}
__device__ __forceinline__ void delay(int ticks) {
  const u64 t0 = __builtin_amdgcn_s_memrealtime();
  while (__builtin_amdgcn_s_memrealtime() - t0 < (u64)ticks) __builtin_amdgcn_s_sleep(1);
}

__device__ __forceinline__ void layernorm8(const float (&hv)[8], u32 (&o)[4]) {
  const float piv = __builtin_bit_cast(float, __builtin_amdgcn_readfirstlane(__builtin_bit_cast(int, hv[0])));
  float s1 = 0.f, s2 = 0.f;
#pragma unroll
  for (int i = 0; i < 8; ++i) { const float d = hv[i] - piv; s1 += d; s2 = __builtin_fmaf(d, d, s2); }
  s1 += dpp<XOR1>(s1); s2 += dpp<XOR1>(s2);
  s1 += dpp<XOR2>(s1); s2 += dpp<XOR2>(s2);
  s1 += dpp<ROR4>(s1); s2 += dpp<ROR4>(s2);
  s1 += dpp<ROR8>(s1); s2 += dpp<ROR8>(s2);
  s1 = x16_32_sum(s1) * (1.f / 512); s2 = x16_32_sum(s2) * (1.f / 512);
  const float var = vmax(__builtin_fmaf(-s1, s1, s2), 0.f);
  const float mb = rbf(piv + s1), rden = __builtin_amdgcn_rcpf(rbf(__builtin_amdgcn_sqrtf(rbf(rbf(var) + 1e-5f))));
#pragma unroll
  for (int i = 0; i < 4; ++i) {
    const u32 d = cvtpk(hv[2 * i] - mb, hv[2 * i + 1] - mb);
    o[i] = cvtpk(bflo(d) * rden, bfhi(d) * rden);
  }
}

__device__ __forceinline__ u32x2 tr_read(char* smem, int off) {
  return __builtin_bit_cast(u32x2, __builtin_amdgcn_ds_read_tr16_b64_v4i16((LAS s16x4*)(smem + off)));
}
__device__ __forceinline__ u32x4 tr_frag(char* smem, int off, int str, int g, int n) {
  const int o = off + (8 * g + (n >> 2)) * str + (n & 3) * 8;
  const u32x2 a = tr_read(smem, o), b = tr_read(smem, o + 4 * str);
  return u32x4{a[0], a[1], b[0], b[1]};
}

__global__ void __launch_bounds__(NTHR, 2) fused_block(Params p) {
  __shared__ __attribute__((aligned(16))) char smem[LDS_BYTES];
  const int c = blockIdx.x, tid = threadIdx.x, lane = tid & 63, w = __builtin_amdgcn_readfirstlane(tid >> 6);
  const int g = lane >> 4, n = lane & 15;
  const int grp = c >> 3, split = (c + grp) & 7;
  const int pB = 7 * (c & 7) + (grp >> 2);
  const bool roleB = (split & 3) == 3 && (grp >> 2) < 7;
  const int b = grp >> 3, kvh = grp & 7;
  const bool roleA = (split & 1) == 0, roleQ = (split & 3) == 1;
  const int ridx = grp * 4 + (split >> 1);
  constexpr int NT_B = 256 - 2 * NT_A - NT_Q;
  const int ntiles = roleA ? NT_A : roleQ ? NT_Q : NT_B;
  static_assert(TORD != 2 || (NT_A == 65 && NT_Q == 62) || (NT_A == 64 && NT_Q == 64), "tile order 2");
  const int tile0 = TORD == 2 ? 64 * split : 256 * (split >> 2) + ((split & 3) > 0 ? NT_A : 0) + ((split & 3) > 1 ? NT_Q : 0) + ((split & 3) > 2 ? NT_A : 0);
  STAMP_DECL;
  STAMP(0);

  const rsrc_t rws = mk_rsrc(p.ws, WS_END);
  const rsrc_t rx = mk_rsrc(p.x, 4096);
  const rsrc_t rk = mk_rsrc(p.k, 128u << 20);
  const rsrc_t rv = mk_rsrc(p.v, 128u << 20);

  const u64 tstart = POLL_T0 ? __builtin_amdgcn_s_memrealtime() : 0;
  u32x2 eraw = {0u, 0u};
  if (w == 0) eraw = ld64_sc1(rws, WS_E);
  const int kvo = (4 * (n & 3) + (n >> 2)) * 256 + g * 16, vvo = lane * 16;
  const int sbase = grp * 16384 * 256 + tile0 * 8192;
  u32x4 K0[8], V0[8], K1[8], V1[8];
  auto tile_so = [&](int tile) { int so = __builtin_amdgcn_readfirstlane(sbase + tile * 8192); asm volatile("" : "+s"(so)); return so; };
  auto ld1K = [&](int j, int so) { return ld128<AUX_NT>(rk, kvo + (j & 3) * 64, so + (j >> 2) * 4096); };
  auto ld1V = [&](int j, int so) { return ld128<AUX_NT>(rv, vvo + (j & 3) * 1024, so + (j >> 2) * 4096); };
  auto ldKV = [&](u32x4 (&kk)[8], u32x4 (&vv)[8], int so, u32 bytes) {
    const rsrc_t rk2 = mk_rsrc(p.k, bytes), rv2 = mk_rsrc(p.v, bytes);
#pragma unroll
    for (int j = 0; j < 8; ++j) { kk[j] = ld128<AUX_NT>(rk2, kvo + (j & 3) * 64, so + (j >> 2) * 4096); SCHED(); }
#pragma unroll
    for (int j = 0; j < 8; ++j) { vv[j] = ld128<AUX_NT>(rv2, vvo + (j & 3) * 1024, so + (j >> 2) * 4096); SCHED(); }
  };
#define PACED() do { asm volatile("s_waitcnt vmcnt(%0)" :: "i"(PACE - 1) : "memory"); SCHED(); } while (0)

  Tag tag;
  auto lds_tag = [&]() { const u32x2 t = *(const u32x2*)(smem + L_TAG); return Tag{(u32)__builtin_amdgcn_readfirstlane(t[0]), (u32)__builtin_amdgcn_readfirstlane(t[1])}; };
  const int col0 = kvh * 512 + (2 * b + (split >> 2)) * 64;
  u32x4 xr, wqr[8];
  if (roleQ && !AB_NOQ) {
    xr = ld128<0>(rx, lane * 16, (w & 3) * 1024);
    const rsrc_t rwq = mk_rsrc(p.wq, 4u << 20);
#pragma unroll
    for (int i = 0; i < 8; ++i) {
      wqr[i] = ld128<WQAUX>(rwq, (lane >> 3) * 8192 + (lane & 7) * 16, (64 * w + 8 * i) * 8192 + col0 * 2);
    }
    FENCE(); SCHED();
    BAR();
  }
  auto tid_of = [&](int i) { return w >= 4 ? 28 + w + 4 * i : i < 8 ? w + 4 * i : 64 * ((split & 4) + 1 - split) + 30 + ((split >> 1) & 1); };
  const int so0 = tile_so(tid_of(0));
#pragma unroll
  for (int j = 0; j < 4; ++j) { K0[j] = ld1K(j, so0); SCHED(); }
  FENCE(); SCHED();
  if (roleQ && !AB_NOQ) {
    if (w < 4) {
      float hv[8];
#pragma unroll
      for (int i = 0; i < 4; ++i) { hv[2 * i] = bflo(xr[i]); hv[2 * i + 1] = bfhi(xr[i]); }
      u32 o[4];
      layernorm8(hv, o);
      *(u32x4*)(smem + L_HN + w * 1088 + lane * 16) = u32x4{o[0], o[1], o[2], o[3]};
    }
    STAMP(15);
    if (w == 0) { tag = mk_tag(eraw); *(u32x2*)(smem + L_TAG) = u32x2{tag.lo, tag.hi}; }
    char* wl = smem + L_W + w * 8192;
#pragma unroll
    for (int i = 0; i < 8; ++i) *(u32x4*)(wl + i * 1024 + lane * 16) = wqr[i];
    STAMP(1);
    u32x4 wf[8];
#pragma unroll
    for (int i = 0; i < 8; ++i) wf[i] = tr_frag(wl, (i >> 2) * 4096 + (i & 3) * 32, 128, g, n);
    BAR();
    const u32x4 x0 = *(const u32x4*)(smem + L_HN + (n & 3) * 1088 + (64 * w + 8 * g) * 2), x1 = *(const u32x4*)(smem + L_HN + (n & 3) * 1088 + (64 * w + 32 + 8 * g) * 2);
#pragma unroll
    for (int t = 0; t < 4; ++t) {
      const f32x4 qa = mfma16(x1, wf[4 + t], mfma16(x0, wf[t], f32x4{0.f, 0.f, 0.f, 0.f}));
      *(float*)(smem + L_QRED + ((w * 4 + g) * 64 + 16 * t + n) * 4) = pick4(qa, g);
    }
    BAR();
    if (w < 4) {
      tag = lds_tag();
      float q = 0.f;
#pragma unroll
      for (int ww = 0; ww < 8; ++ww) q += *(const float*)(smem + L_QRED + ((ww * 4 + w) * 64 + lane) * 4);
      st64_sc1(gone(rbf(q), tag), rws, WS_Q + (w * 4096 + col0 + lane) * 8);
    }
    BAR();
    STAMP(2);
  }
  FENCE(); SCHED();
#pragma unroll
  for (int j = 4; j < 8; ++j) { if (j >= PACE) PACED(); K0[j] = ld1K(j, so0); SCHED(); }
#pragma unroll
  for (int j = 0; j < 8; ++j) { PACED(); V0[j] = ld1V(j, so0); SCHED(); }
  if (w == 0) {
    STAMP(3);
    tag = mk_tag(eraw);
    *(u32x2*)(smem + L_TAG) = u32x2{tag.lo, tag.hi};
    if (POLL_T0) while (__builtin_amdgcn_s_memrealtime() - tstart < POLL_T0) __builtin_amdgcn_s_sleep(2);
    u32x4 gq[4];
    if (AB_NOQ || AB_NOPOLL) { for (int i = 0; i < 4; ++i) gq[i] = u32x4{0x3c003c00u, 0, 0x3c003c00u, 0}; }
    else sweep<4, 1024>(gq, rws, WS_Q + (b * 4096 + kvh * 512) * 8 + lane * 16, tag);
#pragma unroll
    for (int i = 0; i < 4; ++i) *(u32*)(smem + L_QS + i * 320 + lane * 4) = __builtin_amdgcn_perm(gq[i][2], gq[i][0], 0x07060302u);
    STAMP(4);
    BAR();
  }
  FENCE(); SCHED();
  {
    const int so = tile_so(tid_of(1));
#pragma unroll
    for (int j = 0; j < 8; ++j) { PACED(); K1[j] = ld1K(j, so); SCHED(); }
#pragma unroll
    for (int j = 0; j < 8; ++j) { PACED(); V1[j] = ld1V(j, so); SCHED(); }
  }
  FENCE(); SCHED();
  if (w != 0) { STAMP(3); BAR(); }
  tag = lds_tag();
  u32x4 qf[4];
#pragma unroll
  for (int s = 0; s < 4; ++s) qf[s] = *(const u32x4*)(smem + L_QS + (n & 3) * 320 + s * 64 + g * 16);
  STAMP(5);

  constexpr float SCALE = 0.08838834764831845f, LOG2E = 1.4426950408889634f;
  float m = -INFINITY, l = 0.f;
  f32x4 acc[8];
#pragma unroll
  for (int e = 0; e < 8; ++e) acc[e] = f32x4{0.f, 0.f, 0.f, 0.f};
  auto step = [&](u32x4 (&kk)[8], u32x4 (&vv)[8], bool more, int next, u32 bytes) {
    f32x4 s0 = {0.f, 0.f, 0.f, 0.f}, s1 = {0.f, 0.f, 0.f, 0.f};
#pragma unroll
    for (int s = 0; s < 4; ++s) { s0 = mfma16(kk[s], qf[s], s0); s1 = mfma16(kk[4 + s], qf[s], s1); }
    float t[8];
#pragma unroll
    for (int r = 0; r < 4; r += 2) {
      const u32 a0 = cvtpk(s0[r], s0[r + 1]), a1 = cvtpk(s1[r], s1[r + 1]);
      const u32 b0 = cvtpk(bflo(a0) * SCALE, bfhi(a0) * SCALE), b1 = cvtpk(bflo(a1) * SCALE, bfhi(a1) * SCALE);
      t[r] = bflo(b0); t[r + 1] = bfhi(b0); t[4 + r] = bflo(b1); t[5 + r] = bfhi(b1);
    }
    float tm = vmax(vmax(vmax(t[0], t[1]), vmax(t[2], t[3])), vmax(vmax(t[4], t[5]), vmax(t[6], t[7])));
    tm = x16_32_max(tm);
    if (!none(tm > m)) {
      const float mn = vmax(m, tm);
      const float alpha = __builtin_amdgcn_exp2f((m - mn) * LOG2E);
      l *= alpha;
#pragma unroll
      for (int e = 0; e < 8; ++e) acc[e] *= alpha;
      m = mn;
    }
    float pr[8];
#pragma unroll
    for (int r = 0; r < 8; ++r) { pr[r] = __builtin_amdgcn_exp2f((t[r] - m) * LOG2E); l += pr[r]; }
    const u32x4 pf = {cvtpk(pr[0], pr[1]), cvtpk(pr[2], pr[3]), cvtpk(pr[4], pr[5]), cvtpk(pr[6], pr[7])};
#pragma unroll
    for (int i = 0; i < 4; ++i) {
      u32x4 flo, fhi;
#pragma unroll
      for (int q = 0; q < 4; ++q) {
        flo[q] = __builtin_amdgcn_perm(vv[2 * q + 1][i], vv[2 * q][i], 0x05040100u);
        fhi[q] = __builtin_amdgcn_perm(vv[2 * q + 1][i], vv[2 * q][i], 0x07060302u);
      }
      acc[2 * i] = mfma16(flo, pf, acc[2 * i]);
      acc[2 * i + 1] = mfma16(fhi, pf, acc[2 * i + 1]);
    }
    SCHED();
    if (more) ldKV(kk, vv, next, bytes);
    SCHED();
  };
  const int head = ridx >> 2, quarter = ridx & 3;
  u32x4 wr[8];
  {
    const int cnt = w < 4 ? (ntiles - 32 + 3 - w) >> 2 : 8;
#pragma unroll 1
    for (int i = 2; i < cnt; i += 2) {
      step(K0, V0, true, tile_so(tid_of(i)), 128u << 20);
      step(K1, V1, true, tile_so(tid_of(i + 1 < cnt ? i + 1 : i)), i + 1 < cnt ? 128u << 20 : 0u);
    }
    step(K0, V0, false, 0, 0);
    if (!(cnt & 1)) {
      const rsrc_t rw = mk_rsrc(roleA ? p.wo : p.wd, (AB_NOW || w < 4) ? 0u : roleA ? (4u << 20) : roleB ? 1792u * 512 * 2 : 0u);
      const int vo = roleA ? g * 1024 + n * 16 : lane * 16;
      const int so = roleA ? (head * 128 + 32 * (w & 3)) * 1024 + quarter * 256 : (32 * pB + 8 * (w & 3)) * 1024, st = roleA ? 4096 : 1024;
#pragma unroll
      for (int i = 0; i < 8; ++i) wr[i] = ld128<AUX_NT>(rw, vo, so + i * st);
      SCHED();
      step(K1, V1, false, 0, 0);
    }
  }

  {
    int pofs = WS_P + ((b * 32 + kvh * 4 + (w & 3)) * 8 + split) * PREC + lane * 16;
    int rdo = L_MRG + (w & 3) * 528 + lane * 8, mlo = L_ML + (w & 3) * 64;
    PINV(pofs); PINV(rdo); PINV(mlo);
    l = x16_32_sum(l);
    if (n < 4) {
      char* rec = smem + L_MRG + (w * 4 + n) * 528 + g * 16;
#pragma unroll
      for (int e = 0; e < 8; ++e) *(f32x4*)(rec + e * 64) = acc[e];
      if (g == 0) *(f32x2*)(smem + L_ML + n * 64 + w * 8) = f32x2{m, l};
    }
    BAR();
    STAMP(6);
    if (w < 4) {
      f32x4 ml[4];
      f32x2 ov[8];
#pragma unroll
      for (int i = 0; i < 4; ++i) ml[i] = *(const f32x4*)(smem + mlo + i * 16);
#pragma unroll
      for (int ww = 0; ww < 8; ++ww) ov[ww] = *(const f32x2*)(smem + rdo + ww * 2112);
      SCHED();
      const float M = vmax(vmax(vmax(ml[0][0], ml[0][2]), vmax(ml[1][0], ml[1][2])), vmax(vmax(ml[2][0], ml[2][2]), vmax(ml[3][0], ml[3][2])));
      float L = 0.f, o0 = 0.f, o1 = 0.f, wt[8];
#pragma unroll
      for (int ww = 0; ww < 8; ++ww) wt[ww] = __builtin_amdgcn_exp2f((ml[ww >> 1][2 * (ww & 1)] - M) * LOG2E);
#pragma unroll
      for (int ww = 0; ww < 8; ++ww) { o0 = __builtin_fmaf(wt[ww], ov[ww][0], o0); o1 = __builtin_fmaf(wt[ww], ov[ww][1], o1); }
      st128_sc1(gpair(o0, o1, tag), rws, pofs);
#pragma unroll
      for (int ww = 0; ww < 8; ++ww) L = __builtin_fmaf(wt[ww], ml[ww >> 1][2 * (ww & 1) + 1], L);
      if (lane < 8) st128_sc1(gpair(M, L, tag), rws, pofs + 1024);
    }
  }
  STAMP(7);

  if (roleA) {
    const rsrc_t rout = mk_rsrc(p.out, 4096);
    const int bR = ridx >> 5, tR = ridx & 31, hg = lane >> 3, j = lane & 7;
    const u32 xres = ld32<0>(rx, (bR * 512 + 16 * tR + 2 * j) * 2, 0);
    if (w >= 4) {
#pragma unroll
      for (int i = 0; i < 8; ++i) { const int r = 32 * (w & 3) + 4 * i + g; *(u32x4*)(smem + L_W + (16 * (r & 7) + (r >> 3)) * WOSTR + n * 16) = wr[i]; }
    }
    BAR();
    u32x4 af[4];
#pragma unroll
    for (int s = 0; s < 4; ++s) af[s] = tr_frag(smem, L_W + 32 * s * WOSTR + w * 32, WOSTR, g, n);
    int aofs = WS_A + (g * 32 + quarter * 8 + w) * 4096 + head * 128 + n * 8;
    int obw = L_OB + (w >> 1) * 320 + ((w & 1) * 64 + (hg >> 1) * 16 + 2 * j + (hg & 1)) * 2, obr = L_OB + (n & 3) * 320 + g * 16;
    PINV(aofs); PINV(obw); PINV(obr);
    {
      const int o = WS_P + (((w >> 1) * 32 + head) * 8 + hg) * PREC + j * 16, ov = o + (w & 1) * 512;
      u32x4 gv[5] = {{0, 0, 0, 0}, {0, 0, 0, 0}, {0, 0, 0, 0}, {0, 0, 0, 0}, {0, 0, 0, 0}};
      bool todo = true;
#pragma unroll 1
      for (u32 spin = 0; spin < SPIN_CAP; ++spin) {
        FENCE();
        if (todo) {
          gv[4] = ld128_sc1(rws, o + 1024);
#pragma unroll
          for (int i = 0; i < 4; ++i) gv[i] = ld128_sc1(rws, ov + i * 128);
        }
        todo = (gbad32<5>(gv, tag) | gbad8<5>(gv, tag)) != 0;
        const int missing = __builtin_popcountll(__builtin_amdgcn_ballot_w64(todo));
        if (missing == 0) break;
        if (missing > 16) __builtin_amdgcn_s_sleep(A_SLP);
      }
      STAMP(8);
      const float ms = gval(gv[4][0]);
      const float wt = __builtin_amdgcn_exp2f((ms - max_b345(ms)) * LOG2E);
      float x[8];
#pragma unroll
      for (int i = 0; i < 4; ++i) { x[2 * i] = wt * gval(gv[i][0]); x[2 * i + 1] = wt * gval(gv[i][2]); }
      const float L = sum_b345(wt * gval(gv[4][2]));
      const float y0 = rs32(x[0], x[4]), y1 = rs32(x[1], x[5]), y2 = rs32(x[2], x[6]), y3 = rs32(x[3], x[7]);
      const float z0 = rs16(y0, y2), z1 = rs16(y1, y3);
      const float keep = (hg & 1) ? z1 : z0, give = (hg & 1) ? z0 : z1;
      const float ov1 = (keep + dpp<ROR8>(give)) * __builtin_amdgcn_rcpf(L);
      *(unsigned short*)(smem + obw) = (unsigned short)cvtpk(ov1, ov1);
    }
    BAR();
    u32x4 of[4];
#pragma unroll
    for (int s = 0; s < 4; ++s) of[s] = *(const u32x4*)(smem + obr + s * 64);
    SCHED();
    f32x4 aa = {0.f, 0.f, 0.f, 0.f};
#pragma unroll
    for (int s = 0; s < 4; ++s) aa = mfma16(of[s], af[s], aa);
    st64_sc1(gone(pick4(aa, g), tag), rws, aofs);
    STAMP(9);
    if (w != 0) { STAMP_FLUSH(); return; }
    int hofs = WS_H + (bR * 512 + 16 * tR) * 8 + j * 16, oofs = (bR * 512 + 16 * tR + 2 * j) * 2, yofs = WS_Y + ridx * 7168 + lane * 16;
    PINV(hofs); PINV(oofs); PINV(yofs);
    float h0, h1;
    {
      u32x4 ga[4] = {{0, 0, 0, 0}, {0, 0, 0, 0}, {0, 0, 0, 0}, {0, 0, 0, 0}};
      const int o = WS_A + ridx * 4096 + hg * 512 + j * 16;
      bool todo = true;
#pragma unroll 1
      for (u32 spin = 0; spin < SPIN_CAP; ++spin) {
        FENCE();
        if (todo) {
#pragma unroll
          for (int i = 0; i < 4; ++i) ga[i] = ld128_sc1(rws, o + i * 128);
        }
        todo = (gbad32<4>(ga, tag) | gbad8<4>(ga, tag)) != 0;
        const int missing = __builtin_popcountll(__builtin_amdgcn_ballot_w64(todo));
        if (missing == 0) break;
        if (missing > 16) __builtin_amdgcn_s_sleep(R_SLP);
      }
      STAMP(10);
      float a0 = (gval(ga[0][0]) + gval(ga[1][0])) + (gval(ga[2][0]) + gval(ga[3][0]));
      float a1 = (gval(ga[0][2]) + gval(ga[1][2])) + (gval(ga[2][2]) + gval(ga[3][2]));
      a0 += dpp<ROR8>(a0); a1 += dpp<ROR8>(a1);
      a0 = x16_32_sum(a0); a1 = x16_32_sum(a1);
      const u32 ab = cvtpk(a0, a1), hb = cvtpk(bflo(xres) + bflo(ab), bfhi(xres) + bfhi(ab));
      h0 = bflo(hb); h1 = bfhi(hb);
      if (lane < 8) st128_sc1(gpair(h0, h1, tag), rws, hofs);
    }
    {
      u32x4 gy[7];
      delay(R2_DLY);
      sweep<7, 1024>(gy, rws, yofs, tag);
      STAMP(14);
      float y0 = ((gval(gy[0][0]) + gval(gy[1][0])) + (gval(gy[2][0]) + gval(gy[3][0]))) + ((gval(gy[4][0]) + gval(gy[5][0])) + gval(gy[6][0]));
      float y1 = ((gval(gy[0][2]) + gval(gy[1][2])) + (gval(gy[2][2]) + gval(gy[3][2]))) + ((gval(gy[4][2]) + gval(gy[5][2])) + gval(gy[6][2]));
      y0 += dpp<ROR8>(y0); y1 += dpp<ROR8>(y1);
      y0 = x16_32_sum(y0); y1 = x16_32_sum(y1);
      const u32 yb = cvtpk(y0, y1);
      if (lane < 8) __builtin_amdgcn_raw_buffer_store_b32(cvtpk(h0 + bflo(yb), h1 + bfhi(yb)), rout, oofs, 0, 0);
      if (ridx == 0 && lane == 0) {
        const u64 e1 = (((u64)eraw[1] << 32) | eraw[0]) + 1ull;
        st64_sc1(u32x2{(u32)e1, (u32)(e1 >> 32)}, rws, WS_E);
      }
    }
  } else if (roleB) {
    {
      const rsrc_t rwg = mk_rsrc(p.wg, AB_NOW ? 0 : 512 * 1792 * 2), rwu = mk_rsrc(p.wu, AB_NOW ? 0 : 512 * 1792 * 2);
      u32x4 wg[4], wu[4];
      if (B_BAR) __builtin_amdgcn_s_barrier();
#pragma unroll
      for (int i = 0; i < 4; ++i) wg[i] = ld128<AUX_NT>(rwg, (lane >> 2) * 3584 + (lane & 3) * 16, (64 * w + 16 * i) * 3584 + pB * 64);
#pragma unroll
      for (int i = 0; i < 4; ++i) wu[i] = ld128<AUX_NT>(rwu, (lane >> 2) * 3584 + (lane & 3) * 16, (64 * w + 16 * i) * 3584 + pB * 64);
      if (w >= 4) {
#pragma unroll
        for (int i = 0; i < 8; ++i) *(u32x4*)(smem + L_WD + (8 * (w & 3) + i) * WDSTR + lane * 16) = wr[i];
      }
#pragma unroll
      for (int i = 0; i < 4; ++i) {
        *(u32x4*)(smem + L_WG + (4 * w + i) * 1024 + lane * 16) = wg[i];
        *(u32x4*)(smem + L_WU + (4 * w + i) * 1024 + lane * 16) = wu[i];
      }
    }
    BAR();
    u32x4 df[4];
#pragma unroll
    for (int i = 0; i < 4; ++i) df[i] = tr_frag(smem, L_WD + (4 * w + i) * 32, WDSTR, g, n);
    int yofs = WS_Y + (g * 32 + 4 * w) * 7168 + pB * 128 + n * 8, acr = L_ACT + (n & 3) * 64 + g * 16;
    PINV(yofs); PINV(acr);
    if (w < 4) {
      u32x4 gf[16];
      const int wb = ((n & 2) ? L_WU : L_WG) + (8 * g + (n >> 2)) * 64 + w * 16 + (n & 1) * 8;
#pragma unroll
      for (int s = 0; s < 16; ++s) {
        const u32x2 a = tr_read(smem, wb + s * 2048), c2 = tr_read(smem, wb + s * 2048 + 256);
        gf[s] = u32x4{a[0], a[1], c2[0], c2[1]};
      }
      int hnw = L_HN + w * 1088 + lane * 4, hnr = L_HN + (n & 3) * 1088 + g * 16, acw = L_ACT + g * 64 + (8 * w + (n & 7)) * 2;
      PINV(hnw); PINV(hnr); PINV(acw);
#pragma unroll 1
      for (u32 spin = 0; spin < SPIN_CAP; ++spin) {
        FENCE();
        u32x2 sg = {0u, tag.lo};
        if (lane < 8) sg = ld64_sc1(rws, WS_A + (pB + 56 * (w & 1)) * 4096 + (w >> 1) * 128 + lane * 512);
        if (none(sg[1] != tag.lo)) break;
        __builtin_amdgcn_s_sleep(B_SLP);
      }
      STAMP(11);
      if (H_DLY) delay(H_DLY);
      u32x4 gh[4];
      sweep<4, 1024>(gh, rws, WS_H + w * 4096 + lane * 16, tag);
      STAMP(12);
      float hv[8];
#pragma unroll
      for (int i = 0; i < 4; ++i) { hv[2 * i] = gval(gh[i][0]); hv[2 * i + 1] = gval(gh[i][2]); }
      u32 o[4];
      layernorm8(hv, o);
#pragma unroll
      for (int i = 0; i < 4; ++i) *(u32*)(smem + hnw + i * 256) = o[i];
      BAR();
      u32x4 hf[16];
#pragma unroll
      for (int s = 0; s < 16; ++s) hf[s] = *(const u32x4*)(smem + hnr + s * 64);
      SCHED();
      f32x4 ga = {0.f, 0.f, 0.f, 0.f};
#pragma unroll
      for (int s = 0; s < 16; ++s) ga = mfma16(hf[s], gf[s], ga);
      const float gs = rbf(pick4(ga, g)), us = dpp<ROR8>(gs);
      const float sg = rbf(gs * __builtin_amdgcn_rcpf(1.f + __builtin_amdgcn_exp2f(-gs * LOG2E)));
      if (n < 8) *(unsigned short*)(smem + acw) = (unsigned short)cvtpk(sg * us, 0.f);
    }
    if (w >= 4) BAR();
    BAR();
    const u32x4 actf = *(const u32x4*)(smem + acr);
    f32x4 y[4];
#pragma unroll
    for (int i = 0; i < 4; ++i) y[i] = mfma16(actf, df[i], f32x4{0.f, 0.f, 0.f, 0.f});
    SCHED();
#pragma unroll
    for (int i = 0; i < 4; ++i) st64_sc1(gone(pick4(y[i], g), tag), rws, yofs + i * 7168);
    STAMP(13);
  }
  STAMP_FLUSH();
}

extern "C" void kernel_launch(void* const* d_in, const int* in_sizes, int n_in, void* d_out, int out_size, void* d_ws, size_t ws_size,
                              hipStream_t stream) {
  Params p;
  p.x = d_in[0]; p.k = d_in[1]; p.v = d_in[2]; p.wq = d_in[3]; p.wo = d_in[4];
  p.wg = d_in[6]; p.wu = d_in[7]; p.wd = d_in[8];
  p.out = d_out; p.ws = d_ws;
  fused_block<<<NWG, NTHR, 0, stream>>>(p);
}
```
